```python
import torch

def _softmax(x, axis=-1):
    return torch.softmax(x, dim=axis)
def _gelu(x):
    return torch.nn.functional.gelu(x, approximate="tanh")
def _silu(x):
    return torch.nn.functional.silu(x)
def _sigmoid(x):
    return torch.sigmoid(x)
def _layernorm(x, axis=-1, eps=1e-5):
    m = x.mean(dim=axis, keepdim=True)
    v = x.var(dim=axis, keepdim=True, unbiased=False)
    return (x - m) / torch.sqrt(v + eps)
def _scatter_add(tgt, idx, src, axis=0):
    return tgt.clone().index_add_(axis, idx, src)
def _segment_sum(src, seg, n):
    out = torch.zeros((n,) + src.shape[1:], dtype=src.dtype)
    return out.index_add_(0, seg, src)
def _pad(x, pw):
    flat = tuple(v for pair in reversed(pw) for v in pair)
    return torch.nn.functional.pad(x, flat)

kernel_name = 'recurrent_scan__ssm_conv_h3__mixer_s_mlp__prompt'

def setup_inputs():

    _w = (lambda a: (lambda t: t.to(torch.bfloat16) if t.is_floating_point() else t)(torch.as_tensor(a)))
    def _A_setup():
        B, S, D = 1, 2048, 576
        ks = 4
        return {
            "q": _w((lambda *s: torch.randn(*s, dtype=torch.bfloat16) * 0.05)(B, S, D)),
            "k": _w((lambda *s: torch.randn(*s, dtype=torch.bfloat16) * 0.2)(B, S, D)),
            "v": _w((lambda *s: torch.randn(*s, dtype=torch.bfloat16) * 0.2)(B, S, D)),
            "shift_k": _w((lambda *s: torch.randn(*s, dtype=torch.bfloat16) * 0.2)(D, ks) * 0.5),
            "ssm_k": _w((lambda *s: torch.randn(*s, dtype=torch.bfloat16) * 0.2)(D, S) * (1.0 / S ** 0.5)),
            "ssm_D": _w((lambda *s: torch.randn(*s, dtype=torch.bfloat16) * 0.2)(D)),
        }
    _A_in = _A_setup()

    def _B_setup():
        B, S, D, F = 1, 2048, 576, 4608
        return {
            "x":  _w((lambda *s: torch.randn(*s, dtype=torch.bfloat16) * 0.05)(B, S, D)),
            "w1": _w((lambda *s: torch.randn(*s, dtype=torch.bfloat16) * 3.2)(D, D) * (D ** -0.5)),
            "w2": _w((lambda *s: torch.randn(*s, dtype=torch.bfloat16) * 3.2)(D, D) * (D ** -0.5)),
            "w3": _w((lambda *s: torch.randn(*s, dtype=torch.bfloat16) * 3.2)(D, F) * (D ** -0.5)),
            "w4": _w((lambda *s: torch.randn(*s, dtype=torch.bfloat16) * 3.2)(F, D) * (F ** -0.5)),
        }
    _B_in = _B_setup()

    return {
        "A_q": _A_in["q"],
        "A_k": _A_in["k"],
        "A_v": _A_in["v"],
        "A_shift_k": _A_in["shift_k"],
        "A_ssm_k": _A_in["ssm_k"],
        "A_ssm_D": _A_in["ssm_D"],
        "B_x": _B_in["x"],
        "B_w1": _B_in["w1"],
        "B_w2": _B_in["w2"],
        "B_w3": _B_in["w3"],
        "B_w4": _B_in["w4"],
    }

def reference(A_q, A_k, A_v, A_shift_k, A_ssm_k, A_ssm_D, B_x, B_w1, B_w2, B_w3, B_w4):
    def _A_ref(q, k, v, shift_k, ssm_k, ssm_D):
        B, S, D = q.shape
        ks = shift_k.shape[-1]
        k_pad = _pad(k, ((0, 0), (ks - 1, 0), (0, 0)))
        k_shift = torch.zeros_like(k)
        for t in range(ks):
            k_shift = k_shift + k_pad[:, t:t + S, :] * shift_k[:, ks - 1 - t][None, None, :]
        kv = k_shift * v
        L = 2 * S
        kv_pad = _pad(kv, ((0, 0), (0, S), (0, 0)))
        kf_pad = _pad(ssm_k, ((0, 0), (0, S)))
        kv_f = (lambda x, axes=(-2,-1): torch.fft.rfft2(x.float(), dim=axes))(kv_pad[:, None, :, :], axes=(1, 2))
        kf_f = (lambda x, axes=(-2,-1): torch.fft.rfft2(x.float(), dim=axes))(kf_pad[:, None, :], axes=(1, 2))
        kf_f = (lambda x, *d: x.permute(*d))(kf_f, 1, 2, 0)
        y_f = kv_f * kf_f[None, :, :, :]
        y = (lambda x, s, axes=(-2,-1): torch.fft.irfft2(x, s=s, dim=axes).to(torch.bfloat16))(y_f, (1, L), axes=(1, 2))[:, 0, :S, :]
        y = y + kv * ssm_D[None, None, :]
        return q * y

    def _B_ref(x, w1, w2, w3, w4):
        B, S, D = x.shape
        h = _gelu(_layernorm(x, axis=-1) @ w1)
        c = D // 4
        g0 = _pad(h[:, :, 0*c:1*c], ((0, 0), (1, 0), (0, 0)))[:, :S, :]
        g1 = _pad(h[:, :, 1*c:2*c], ((0, 0), (0, 1), (0, 0)))[:, 1:, :]
        g2 = _pad(h[:, :, 2*c:3*c], ((0, 0), (2, 0), (0, 0)))[:, :S, :]
        g3 = _pad(h[:, :, 3*c:   ], ((0, 0), (0, 2), (0, 0)))[:, 2:, :]
        hs = torch.concatenate([g0, g1, g2, g3], axis=-1) @ w2
        x1 = x + hs
        h2 = _gelu(_layernorm(x1, axis=-1) @ w3) @ w4
        return x1 + h2

    x = A_q
    xn = _layernorm(x, axis=-1)
    s = _A_ref(xn, A_k, A_v, A_shift_k, A_ssm_k, A_ssm_D)
    g = _silu(_B_ref(xn, B_w1, B_w2, B_w3, B_w4))
    return x + s * g
```

```cpp
#include <hip/hip_runtime.h>
#include <hip/hip_bf16.h>
#include <utility>

typedef __attribute__((ext_vector_type(2))) __bf16 bf16x2;
typedef __attribute__((ext_vector_type(4))) __bf16 bf16x4;
typedef __attribute__((ext_vector_type(8))) __bf16 bf16x8;
typedef __attribute__((ext_vector_type(2))) float f32x2;
typedef __attribute__((ext_vector_type(4))) float f32x4;
typedef __attribute__((ext_vector_type(2))) unsigned u32x2;
typedef __attribute__((ext_vector_type(4))) unsigned u32x4;
#define LDS3 __attribute__((address_space(3)))
#define DEV __device__ __forceinline__
#define SB() __builtin_amdgcn_sched_barrier(0)

constexpr int S_ = 2048, D_ = 576, F_ = 4608;
constexpr float EPS = 1e-5f;
constexpr float GK = 2.302208198f;
constexpr float GC = 0.044715f;
constexpr size_t SZ_ACT = (size_t)S_ * D_ * 2;
constexpr size_t O_HS = 0, O_XC = O_HS + SZ_ACT, O_KVT = O_XC + SZ_ACT, O_YT = O_KVT + SZ_ACT,
                 O_P = O_YT + SZ_ACT, O_H = O_P + 4 * SZ_ACT, O_XST = O_H + 8 * SZ_ACT,
                 O_MU = O_XST + 16384, O_SS = O_MU + 8192, O_RM = O_SS + 131072, O_E = O_RM + 2048, O_F = O_E + 1024, O_PE = O_P + 3 * SZ_ACT;
constexpr int F1 = 0, FC = 1024, F2 = 2048, F3 = 4096, F4 = F3 + 4 * 2048;

template <class F, int... I> DEV void sfor_(F&& f, std::integer_sequence<int, I...>) { (f(std::integral_constant<int, I>{}), ...); }
template <int N, class F> DEV void sfor(F&& f) { sfor_(f, std::make_integer_sequence<int, N>{}); }

DEV void dma(unsigned lds, const char* g, unsigned voff) {
  asm volatile("s_mov_b32 m0, %0\n\ts_nop 0\n\tglobal_load_lds_dwordx4 %1, %2" ::"s"(lds), "v"(voff), "s"(g) : "memory");
}
template <int N> DEV void waitv() { asm volatile("s_waitcnt vmcnt(%0)" ::"n"(N) : "memory"); }
DEV void bar() { asm volatile("s_barrier" ::: "memory"); }
DEV void barl() { asm volatile("s_waitcnt lgkmcnt(0)\n\ts_barrier" ::: "memory"); }
template <int O> DEV void ld8(u32x2& d, const char* g, unsigned voff) { asm volatile("global_load_dwordx2 %0, %1, %2 offset:%3" : "=v"(d) : "v"(voff), "s"(g), "n"(O) : "memory"); }
template <class T> DEV void ld16(T& d, const char* g, unsigned voff) { asm volatile("global_load_dwordx4 %0, %1, %2" : "=v"(d) : "v"(voff), "s"(g) : "memory"); }
template <int N, class T> DEV void syncb(T& a, T& b) { asm volatile("s_waitcnt vmcnt(%2)\n\ts_barrier" : "+v"(a), "+v"(b) : "n"(N) : "memory"); }
#define TS(i)
#define TSDECL
#define TSEND
DEV void dmac(unsigned lds, const char* g, unsigned voff) {
  asm volatile("s_mov_b32 m0, %0\n\ts_nop 0\n\tglobal_load_lds_dwordx4 %1, %2 sc1" ::"s"(lds), "v"(voff), "s"(g) : "memory");
}
template <int O> DEV void ld4(unsigned& d, const char* g, unsigned voff) { asm volatile("global_load_dword %0, %1, %2 offset:%3" : "=v"(d) : "v"(voff), "s"(g), "n"(O) : "memory"); }
template <int O> DEV void ld4c(unsigned& d, const char* g, unsigned voff) { asm volatile("global_load_dword %0, %1, %2 offset:%3 sc1" : "=v"(d) : "v"(voff), "s"(g), "n"(O) : "memory"); }
template <int O> DEV void ld8c(u32x2& d, const char* g, unsigned voff) { asm volatile("global_load_dwordx2 %0, %1, %2 offset:%3 sc1" : "=v"(d) : "v"(voff), "s"(g), "n"(O) : "memory"); }
template <class T> DEV void ld16c(T& d, const char* g, unsigned voff) { asm volatile("global_load_dwordx4 %0, %1, %2 sc1" : "=v"(d) : "v"(voff), "s"(g) : "memory"); }
DEV void st4c(const char* g, unsigned voff, unsigned d) { asm volatile("global_store_dword %0, %1, %2 sc1" ::"v"(voff), "v"(d), "s"(g) : "memory"); }
DEV void st8c(const char* g, unsigned voff, u32x2 d) { asm volatile("global_store_dwordx2 %0, %1, %2 sc1" ::"v"(voff), "v"(d), "s"(g) : "memory"); }
template <int O = 0> DEV void st16c(const char* g, unsigned voff, u32x4 d) { asm volatile("global_store_dwordx4 %0, %1, %2 offset:%3 sc1\n\ts_nop 1" ::"v"(voff), "v"(d), "s"(g), "n"(O) : "memory"); }
DEV bool allq(unsigned v, unsigned tag) { return __builtin_amdgcn_ballot_w64(v != tag) == 0; }
DEV bool allq(u32x4 v, unsigned tag) { return __builtin_amdgcn_ballot_w64(((v[0] ^ tag) | (v[1] ^ tag) | (v[2] ^ tag) | (v[3] ^ tag)) != 0) == 0; }
template <class T> DEV void spin(const char* g, unsigned voff, unsigned tag) {
#pragma unroll 1
  for (int it = 0; it < (1 << 17); ++it) {
    T v;
    if constexpr (sizeof(T) == 4) asm volatile("global_load_dword %0, %1, %2 sc1\n\ts_waitcnt vmcnt(0)" : "=&v"(v) : "v"(voff), "s"(g) : "memory");
    else asm volatile("global_load_dwordx4 %0, %1, %2 sc1\n\ts_waitcnt vmcnt(0)" : "=&v"(v) : "v"(voff), "s"(g) : "memory");
    if (allq(v, tag)) break;
    __builtin_amdgcn_s_sleep(2);
  }
}
template <class T> DEV void pin(T& d) { asm volatile("" : "+v"(d)); }

DEV bf16x8 trfrag(unsigned a, int second) {
  bf16x4 x = __builtin_amdgcn_ds_read_tr16_b64_v4bf16((LDS3 bf16x4*)a);
  bf16x4 y = __builtin_amdgcn_ds_read_tr16_b64_v4bf16((LDS3 bf16x4*)(a + second));
  return __builtin_shufflevector(x, y, 0, 1, 2, 3, 4, 5, 6, 7);
}
DEV bf16x8 ldsfrag(unsigned a) { return *(LDS3 bf16x8*)a; }
DEV f32x4 mfma(bf16x8 a, bf16x8 b, f32x4 c) { return __builtin_amdgcn_mfma_f32_16x16x32_bf16(a, b, c, 0, 0, 0); }
DEV unsigned pk2(float a, float b) { f32x2 v = {a, b}; return __builtin_bit_cast(unsigned, __builtin_convertvector(v, bf16x2)); }
DEV float blo(unsigned u) { return __builtin_bit_cast(float, u << 16); }
DEV float bhi(unsigned u) { return __builtin_bit_cast(float, u & 0xffff0000u); }
DEV float gelu(float u, float ca, float cb, float sg) {
  float t = u * u; float p = __builtin_fmaf(t, cb, ca); float e = __builtin_amdgcn_exp2f(u * p);
  float o = u * __builtin_amdgcn_rcpf(__builtin_fmaf(e, sg, sg)); pin(o); return o;
}
template <int C> DEV float dpp(float v) { return __builtin_bit_cast(float, __builtin_amdgcn_update_dpp(0, __builtin_bit_cast(int, v), C, 0xf, 0xf, true)); }
DEV float sum16(float v) { v += dpp<0x128>(v); v += dpp<0x124>(v); v += dpp<0x122>(v); v += dpp<0x121>(v); return v; }
DEV float sumq(float v) { v += __shfl_xor(v, 16); v += __shfl_xor(v, 32); return v; }

DEV bf16x8 splitfrag(unsigned a) { const u32x2 x = *(LDS3 u32x2*)a, y = *(LDS3 u32x2*)(a + 512); return __builtin_bit_cast(bf16x8, u32x4{x[0], x[1], y[0], y[1]}); }
template <int I> using IC = std::integral_constant<int, I>;

constexpr int L_CV = 73728, CV_SZ = 8736, CV_U = 512, CV_H = 4640, L_EXT = 100352, L_ACT = 118784, L_RM = 156672, L_SP = 147456, L_JK = 159744;
DEV void pf(unsigned L, const char* g, unsigned voff) { asm volatile("s_mov_b32 m0, %0\n\ts_nop 0\n\tglobal_load_lds_dword %1, %2" ::"s"(L + L_JK), "v"(voff), "s"(g) : "memory"); }
DEV bf16x8 toep(unsigned a, unsigned sh) {
  const LDS3 unsigned* hp = (const LDS3 unsigned*)a;
  const unsigned d0 = hp[0], d1 = hp[1], d2 = hp[2], d3 = hp[3], d4 = hp[4];
  return __builtin_bit_cast(bf16x8, u32x4{__builtin_amdgcn_alignbyte(d1, d0, sh), __builtin_amdgcn_alignbyte(d2, d1, sh), __builtin_amdgcn_alignbyte(d3, d2, sh), __builtin_amdgcn_alignbyte(d4, d3, sh)});
}
constexpr int imin(int a, int b) { return a < b ? a : b; }
constexpr int imax(int a, int b) { return a > b ? a : b; }
constexpr int endidx(int wi) { return wi < 0 ? 0 : wi < 18 ? 2 * wi + 2 : wi > 40 ? 59 : 19 + wi; }
constexpr int lim3(int G) { return imin(59, 18 + endidx(2 * G - 2)); }
constexpr int phys(int n) { return ((n % 18) & 1) ? (n % 18) >> 1 : 9 + ((n % 18) >> 1); }
constexpr int slotIt(int n) { int G = 1; while (lim3(G) <= n) ++G; return 8 * G - 3 + 2 * (n - lim3(G - 1)); }
constexpr int PUB[4] = {124, 176, 212, 230}, LDF[5] = {143, 190, 226, 250, 216}, EXM[5] = {156, 203, 239, 266, 239};
constexpr int gI(int v) { return v < 32 ? 74 + v * 4 / 3 : v < 48 ? 118 + (v - 32) * 11 / 4 : 162 + (v - 48) * 5 / 2; }
constexpr int gV(int n) { for (int v = 0; v < 72; ++v) if (gI(v) == n) return v; return -1; }
constexpr int wIt(int j, int k) { return j < 5 ? 109 + 8 * j + 2 * k : 162 + 9 * (j - 5) + 2 + k; }
constexpr int bIt(int j, int k) { return j < 2 ? PUB[0] + 2 + 2 * j + k : j < 4 ? EXM[0] - 3 + 2 * j + k : 162 + 9 * (j - 4) + k; }
constexpr int eIt(int e) { return e < 2 ? 101 + 2 * e : 162 + 9 * (4 * e - 4) + 6; }
constexpr int nops(int n) {
  if (n < 0) return n != -2;
  int c = 0;
  for (int i = 18; i < 59; ++i) c += slotIt(i) == n;
  for (int j = 0; j < 18; ++j) for (int k = 0; k < 2; ++k) c += (wIt(j, k) == n) + (bIt(j, k) == n) * (j < 16 ? 1 : 2);
  for (int e = 0; e < 5; ++e) c += (eIt(e) == n) + (LDF[e] == n);
  const int v = gV(n);
  return c + (v >= 0 && (v < 64 ? (v & 7) == 7 : (v & 3) == 3));
}
constexpr int need4(int j) { return imax(imax(wIt(j, 1), bIt(j, 1)), eIt(j / 4)); }
constexpr int vcnt(int n) {
  int l = 0;
  if (n < 162) { const int e = endidx(2 * ((n + 4) / 8) + 1); l = e <= 18 ? -12 + e / 2 : slotIt(e - 1); if (n == 156) l = imax(l, need4(0)); }
  else l = need4((n - 162) / 9 + 1);
  for (int e = 0; e < 5; ++e) { if (e < 4 && PUB[e] == n) l = imax(l, gI(e < 3 ? 31 + 16 * e : 71)); if (EXM[e] == n) l = imax(l, LDF[e]); }
  int c = 0;
  for (int m = l + 1; m < n; ++m) c += nops(m);
  return c;
}

__global__ __launch_bounds__(512, 2) void k_all(const char* __restrict__ x, const char* __restrict__ kk, const char* __restrict__ vv, const char* __restrict__ shk,
    const char* __restrict__ ssk, const unsigned* __restrict__ ssd, const char* __restrict__ w1, const char* __restrict__ w2, const char* __restrict__ w3,
    const char* __restrict__ w4, char* __restrict__ ws, char* __restrict__ out) {
  __shared__ __attribute__((aligned(1024))) char smem[163840];
  const unsigned L = (unsigned)(size_t)(LDS3 char*)smem;
  const int b = blockIdx.x, tid = threadIdx.x, lane = tid & 63, w = __builtin_amdgcn_readfirstlane(tid >> 6);
  const int g = b & 3, T = ((b >> 2) & 1) * 8 + (b >> 5), s = (b >> 3) & 3, T32 = 4 * T + s, xi = b >> 3;
  const int q = lane >> 4, c = lane & 15, tf = w >> 2, p = w & 3, row = 4 * q + (c >> 2);
  const char* fl = ws + O_F;
  const int fid = (T * 32 + g * 4 + s) * 4, ch3 = 512 + (b >> 2);
  TSDECL;
  unsigned tag;
  ld4c<0>(tag, ws + O_E, 0u);
  const unsigned vz = lane * 16;
  const bool ext = p == tf;
  const int rr = 4 * w + q;
  const unsigned vo = rr * 1152 + ((c ^ ((rr & 7) << 1)) * 16), ve = (lane >> 1) * 1152 + (lane & 1) * 16;
  const unsigned t0 = L + row * 256 + (((2 * p) ^ (row & 7)) * 32) + (c & 3) * 8, t1 = t0 ^ 32, te = L + L_EXT + row * 32 + (c & 3) * 8;
  const char* wm2 = w2 + 256 * g;
  {
    const int r = tid >> 4, srow = 32 * T32 + r + ((g & 1) ? 1 : -1) * (1 + (g >> 1));
    const bool ok = srow >= 0 && srow < S_;
    u32x2 xv[9];
    { const unsigned xo = (unsigned)(ok ? srow : 0) * 1152 + c * 8; sfor<9>([&](auto I) { ld8<I * 128>(xv[I], x, xo); }); }
    const char* wm = w1 + 288 * g + 32 * (g & 1);
    const char* we = w1 + 288 * g + ((g & 1) ? 0 : 256);
    sfor<3>([&](auto I) { const int n = min(8 * I + w, 17); dma(L + L_EXT + n * 1024, we + (size_t)n * 32 * 1152, ve); });
    sfor<12>([&](auto I) { dma(L + I * 8192 + w * 1024, wm + (size_t)I * 32 * 1152, vo); });
    unsigned ar[19]; u32x4 sk;
    const bool kw = w < 5;
    const int ki = min(tid, 287), ko = ki / 72, kd = 144 * g + 2 * (ki - ko * 72), kt = 32 * T32 + 8 * ko;
    const int rmr = (q < 2 || b >= 32) ? 2 * b + (q & 1) : 510 + 2 * b + q;
    { const int str = kw ? 1152 : 64, o0 = kw ? (kt - 3) * 1152 + kd * 2 : rmr * 1152 + c * 4, o1 = o0 + (kw ? 3 * 1152 : 11 * 64);
      const char *b0 = kw ? kk : w2, *b1 = kw ? vv : w2, *b2 = kw ? vv : w == 5 ? w2 : ssk;
      sfor<11>([&](auto I) { ld4<0>(ar[I], b0, I < 3 ? max(o0 + I * str, 0) : o0 + I * str); });
      sfor<7>([&](auto I) { ld4<0>(ar[11 + I], b1, o1 + I * str); });
      const int l3 = min(lane, 53) / 3, l4 = min(lane, 53) - 3 * l3;
      ld4<0>(ar[18], b2, kw ? o1 + 7 * 1152 : w == 5 ? (18 * xi + l3) * 1152 + (l4 < 2 ? 256 * g + 128 * l4 : 1024) : w == 6 ? b * 8192 + lane * 128 : ch3 * 4096 + (lane & 31) * 128);
      ld16(sk, shk, kd * 8); }
    asm volatile("s_waitcnt vmcnt(35)" : "+v"(xv[0]), "+v"(xv[1]), "+v"(xv[2]), "+v"(xv[3]), "+v"(xv[4]), "+v"(xv[5]), "+v"(xv[6]), "+v"(xv[7]), "+v"(xv[8]), "+v"(tag)::"memory");
    TS(1);
    tag += 1;
    float f[36], sm = 0;
#pragma unroll
    for (int i = 0; i < 9; ++i) { f[4 * i] = blo(xv[i][0]); f[4 * i + 1] = bhi(xv[i][0]); f[4 * i + 2] = blo(xv[i][1]); f[4 * i + 3] = bhi(xv[i][1]); sm += (f[4 * i] + f[4 * i + 1]) + (f[4 * i + 2] + f[4 * i + 3]); }
    const float m = sum16(sm) * (1.f / D_);
    float v = 0;
#pragma unroll
    for (int i = 0; i < 36; ++i) { f[i] -= m; v += f[i] * f[i]; }
    v = sum16(v) * (1.f / D_);
    const float rs = __builtin_amdgcn_rsqf(v + EPS);
    const float sc = ok ? rs * __builtin_amdgcn_rsqf(v * rs * rs + EPS) : 0.f;
    const unsigned za = L + L_ACT + r * 1184 + (c >> 3) * 64 + (c & 3) * 16 + ((c >> 2) & 1) * 8;
#pragma unroll
    for (int i = 0; i < 9; ++i) *(LDS3 u32x2*)(za + i * 128) = u32x2{pk2(f[4 * i] * sc, f[4 * i + 1] * sc), pk2(f[4 * i + 2] * sc, f[4 * i + 3] * sc)};
    f32x4 a0 = {}, a1 = {}, ae = {};
    const unsigned bb = L + L_ACT + (16 * tf + c) * 1184 + q * 16;
    TS(2);
    sfor<6>([&](auto J) {
      constexpr int j = J;
      waitv<j == 0 ? 29 : j < 4 ? 26 : 6>();
      barl();
      bf16x8 B[3], A0[3], A1[3];
      sfor<3>([&](auto I) { constexpr int ks = 3 * j + I, so = (ks % 12) * 8192; B[I] = ldsfrag(bb + ks * 64); A0[I] = trfrag(t0 + so, 4096); A1[I] = trfrag(t1 + so, 4096); });
      SB();
      if constexpr (j == 1 || j == 2) sfor<3>([&](auto I) { constexpr int n = 3 * (j - 1) + I; dma(L + n * 8192 + w * 1024, wm + (size_t)(12 + n) * 32 * 1152, vo); });
      if constexpr (j >= 3) sfor<3>([&](auto I) { constexpr int n = 3 * (j - 3) + I; dma(L + ((n + 6) % 12) * 8192 + w * 1024, wm2 + (size_t)n * 32 * 1152, vo); });
      SB();
      if (ext) { bf16x8 E[3]; sfor<3>([&](auto I) { E[I] = trfrag(te + (3 * j + I) * 1024, 512); }); SB(); sfor<3>([&](auto I) { ae = mfma(E[I], B[I], ae); }); }
      sfor<3>([&](auto I) { a0 = mfma(A0[I], B[I], a0); a1 = mfma(A1[I], B[I], a1); });
      if constexpr (j == 4) {
        sfor<19>([&](auto I) { pin(ar[I]); }); pin(sk);
        if (kw) {
          if (kt == 0) ar[0] = ar[1] = ar[2] = 0;
          float r0[8], r1[8];
#pragma unroll
          for (int i = 0; i < 8; ++i) {
            r0[i] = (blo(ar[i + 3]) * blo(sk[0]) + blo(ar[i + 2]) * bhi(sk[0]) + blo(ar[i + 1]) * blo(sk[1]) + blo(ar[i]) * bhi(sk[1])) * blo(ar[11 + i]);
            r1[i] = (bhi(ar[i + 3]) * blo(sk[2]) + bhi(ar[i + 2]) * bhi(sk[2]) + bhi(ar[i + 1]) * blo(sk[3]) + bhi(ar[i]) * bhi(sk[3])) * bhi(ar[11 + i]);
          }
          const unsigned d = (T32 * D_ + kd) * 64 + ko * 16;
          st16c<0>(ws + O_KVT, d, u32x4{pk2(r0[7], r0[6]), pk2(r0[5], r0[4]), pk2(r0[3], r0[2]), pk2(r0[1], r0[0])});
          st16c<64>(ws + O_KVT, d, u32x4{pk2(r1[7], r1[6]), pk2(r1[5], r1[4]), pk2(r1[3], r1[2]), pk2(r1[1], r1[0])});
        } else if (w == 7) {
          float z = 0;
#pragma unroll
          for (int i = 0; i < 18; ++i) z += blo(ar[i]) + bhi(ar[i]);
          z = sum16(z) * (1.f / D_);
          const float z1 = __shfl_xor(z, 16);
          if ((lane & 31) == 0) { const int k = rmr & 31; st4c(ws + O_RM, ((rmr >> 5) * 32 + ((k >> 2) & 3) * 8 + (k >> 4) * 4 + (k & 3)) * 2, pk2(z, z1)); }
        }
      }
    });
    TS(3);
    auto G = [&](float u) { return gelu(u, -GK, -GK * GC, 1.f); };
    const char* hs = ws + O_HS + (size_t)(2 * T32 + tf) * 18 * 1024;
    st16c(hs + (((9 * g + 1) >> 1) + p) * 1024, vz, u32x4{pk2(G(a0[0]), G(a0[1])), pk2(G(a0[2]), G(a0[3])), pk2(G(a1[0]), G(a1[1])), pk2(G(a1[2]), G(a1[3]))});
    if (ext) st8c(hs + ((g & 1) ? 9 * g : 9 * g + 8) * 512, lane * 8, u32x2{pk2(G(ae[0]), G(ae[1])), pk2(G(ae[2]), G(ae[3]))});
    if (g < 2 && ok && c == 0) st8c(ws + O_XST, srow * 8, __builtin_bit_cast(u32x2, f32x2{m, rs}));
    waitv<0>(); bar();
    if (tid == 0) st4c(fl + F1, b * 4, tag);
    TS(4);
  }
  const char* wa = w3 + 2304 * g + 512 * s;
  const unsigned va = rr * 9216 + ((c ^ ((rr & 7) << 1)) * 16);
  {
    const int t = 32 * T32 + 16 * tf + c;
    sfor<3>([&](auto I) { dma(L + (3 + I) * 8192 + w * 1024, wm2 + (size_t)(9 + I) * 32 * 1152, vo); });
    sfor<3>([&](auto I) { const int n = min(8 * I + w, 17); dma(L + L_EXT + n * 1024, w2 + 1024 + 32 * g + (size_t)n * 32 * 1152, ve); });
    u32x2 x0, x1, xe, st;
    { const unsigned xo = t * 1152 + 256 * g + 64 * p + 8 * q; ld8<0>(x0, x, xo); ld8<32>(x1, x, xo); ld8<1024>(xe, x, t * 1152 + 32 * g + 8 * q); }
    bf16x8 A0[18], A1[18];
    sfor<9>([&](auto I) { constexpr int so = ((I + 6) % 12) * 8192; A0[I] = trfrag(t0 + so, 4096); A1[I] = trfrag(t1 + so, 4096); });
    barl();
    sfor<6>([&](auto I) { dma(L + ((6 + I) % 9) * 8192 + w * 1024, wm2 + (size_t)(12 + I) * 32 * 1152, vo); });
    if (w < 3 && lane < 34) *(LDS3 u32x4*)(L + L_CV + w * CV_SZ + (lane < 32 ? lane * 16 : 4096 + lane * 16)) = u32x4{0, 0, 0, 0};
    if (w == 0) spin<u32x4>(fl + F1, vz, tag);
    else { const int i = min(tid - 64, 323), i2 = i / 18; pf(L, w3 + 2304 * g, (18 * xi + i2) * 9216 + (i - 18 * i2) * 128); waitv<1>(); }
    bar();
    TS(5);
    if (b == 0 && tid == 0) st4c(ws + O_E, 0u, tag);
    ld8c<0>(st, ws + O_XST, t * 8);
    sfor<5>([&](auto I) {
      const int n = min(8 * I + w, 37);
      dmac(L + (n < 36 ? L_ACT : L_RM - 36864) + n * 1024, ws + (n < 36 ? O_HS + (size_t)36864 * T32 : O_RM - 36864) + n * 1024, vz);
    });
    sfor<3>([&](auto I) {
      const int ch = I < 2 ? 2 * b + I : ch3;
      dmac(L + L_CV + I * CV_SZ + (tf ? CV_H : CV_U) + p * 1024, tf ? ssk + (size_t)ch * 4096 + p * 1024 : ws + O_KVT + ((size_t)16 * p * D_ + ch) * 64, tf ? vz : (lane >> 2) * 36864 + (lane & 3) * 16);
    });
    sfor<9>([&](auto I) { constexpr int so = ((I + 3) % 9) * 8192; A0[9 + I] = trfrag(t0 + so, 4096); A1[9 + I] = trfrag(t1 + so, 4096); });
    waitv<3>(); barl();
    TS(6);
    f32x4 a0 = {}, a1 = {}, ae = {}, am = {};
    const unsigned bb = L + L_ACT + tf * 18432, tm = L + L_RM + q * 16;
    auto gm = [&](auto X) {
      bf16x8 B[18], M[18], E[18];
      auto rd = [&](auto I) { B[I] = (I == 4 || I == 13) ? splitfrag(bb + I * 1024 + lane * 8) : ldsfrag(bb + I * 1024 + vz); M[I] = ldsfrag(tm + I * 64); if constexpr (X) E[I] = trfrag(te + I * 1024, 512); };
      sfor<3>(rd);
      sfor<18>([&](auto I) {
        if constexpr (I + 3 < 18) rd(IC<I + 3>{});
        a0 = mfma(A0[I], B[I], a0); a1 = mfma(A1[I], B[I], a1); am = mfma(M[I], B[I], am);
        if constexpr (X) ae = mfma(E[I], B[I], ae);
        SB();
      });
    };
    if (ext) gm(IC<1>{}); else gm(IC<0>{});
    TS(7);
    pin(x0); pin(x1); pin(xe); pin(st);
    const f32x2 sf = __builtin_bit_cast(f32x2, st); const float m = sf[0], rs = sf[1], mu = am[0];
    float ss = 0;
    auto E = [&](u32x2 xv, f32x4 a) {
      const float v0 = (blo(xv[0]) - m) * rs + a[0] - mu, v1 = (bhi(xv[0]) - m) * rs + a[1] - mu, v2 = (blo(xv[1]) - m) * rs + a[2] - mu, v3 = (bhi(xv[1]) - m) * rs + a[3] - mu;
      ss += (v0 * v0 + v1 * v1) + (v2 * v2 + v3 * v3);
      return u32x2{pk2(v0, v1), pk2(v2, v3)};
    };
    const char* xc = ws + O_XC + (size_t)(2 * T32 + tf) * 18 * 1024;
    const u32x2 o0 = E(x0, a0), o1 = E(x1, a1);
    st16c(xc + (4 * g + p) * 1024, vz, u32x4{o0[0], o0[1], o1[0], o1[1]});
    if (ext) st8c(xc + (32 + g) * 512, lane * 8, E(xe, ae));
    ss = sumq(ss);
    if (lane < 16) { st4c(ws + O_SS + (2 * T32 + tf) * 1024 + (4 * g + p) * 64, lane * 4, __builtin_bit_cast(unsigned, ss)); if (g == 0 && p == 0) st4c(ws + O_MU, t * 4, __builtin_bit_cast(unsigned, mu)); }
    sfor<9>([&](auto I) { dma(L + I * 8192 + w * 1024, wa + (size_t)I * 32 * 9216, va); });
    pf(L, w4 + (size_t)1327104 * g + xi * 41472, min(tid, 323) * 128);
    waitv<10>();
    if (w == 4 && lane < 3) {
      const unsigned d01 = ssd[b], d3 = ssd[ch3 >> 1];
      LDS3 unsigned short* hp = (LDS3 unsigned short*)(L + L_CV + lane * CV_SZ + CV_H);
      *hp = (unsigned short)(pk2(blo(*hp) + (lane == 0 ? blo(d01) : lane == 1 ? bhi(d01) : (ch3 & 1) ? bhi(d3) : blo(d3)), 0.f) & 0xffff);
    }
    barl();
    if (tid == 0) st4c(fl + F2, fid, tag);
    TS(8);
  }
  f32x4 ys[2];
  {
    const int st = 9 + c - 8 * q;
    const unsigned sh = (st & 1) * 2, ho = L + L_CV + CV_H + (st >> 1) * 4, uo = L + L_CV + CV_U + (c - 1) * 32 + q * 16;
    f32x4 acc[8] = {}, c0 = {}, c1 = {};
    sfor<2>([&](auto S) {
      const unsigned ha = ho + tf * CV_SZ + (2 * p + S) * 64, ua = uo + tf * CV_SZ - (2 * p + S) * 64;
      bf16x8 A[8], B[8];
      sfor<8>([&](auto M) { B[M] = ldsfrag(ua + M * 512); });
      sfor<8>([&](auto M) { A[M] = toep(ha + M * 512, sh); sfor<8 - M>([&](auto I) { acc[M + I] = mfma(A[M], B[I], acc[M + I]); }); });
    });
    { const unsigned ha = ho + 2 * CV_SZ + w * 64, ua = uo + 2 * CV_SZ - w * 64, za = L + L_CV + (lane & 31) * 16;
      sfor<8>([&](auto M) {
        const bf16x8 A = toep(ha + M * 512, sh);
        c1 = mfma(A, ldsfrag(M < 5 || 7 - g >= M ? ua + (7 - g - M) * 512 : za), c1);
        if constexpr (M < 4) c0 = mfma(A, ldsfrag(g >= M ? ua + (g - M) * 512 : za), c0);
      }); }
    TS(9);
    if (w == 0) spin<unsigned>(fl + F2 + T * 128, c * 4, tag);
    barl();
    TS(10);
    const unsigned ra = L + L_CV + c * 64 + q * 16;
    sfor<8>([&](auto I) { *(LDS3 f32x4*)(ra + w * 8192 + I * 1024) = acc[I]; });
    *(LDS3 f32x4*)(ra + 65536 + w * 2048) = c0; *(LDS3 f32x4*)(ra + 65536 + w * 2048 + 1024) = c1;
    barl();
    const unsigned rb = L + L_CV + lane * 32;
    ys[0] = ys[1] = f32x4{};
    sfor<4>([&](auto I) { ys[0] += *(LDS3 f32x4*)(rb + (4 * tf + I) * 8192 + p * 2048); ys[1] += *(LDS3 f32x4*)(rb + (4 * tf + I) * 8192 + p * 2048 + 16); });
    if (w == 7) {
      f32x4 y0 = {}, y1 = {};
      sfor<8>([&](auto I) { y0 += *(LDS3 f32x4*)(rb + 65536 + I * 2048); y1 += *(LDS3 f32x4*)(rb + 65536 + I * 2048 + 16); });
      st16c(ws + O_YT + (size_t)ch3 * 4096, (lane < 32 ? g : 7 - g) * 512 + (lane & 31) * 16, u32x4{pk2(y0[0], y0[1]), pk2(y0[2], y0[3]), pk2(y1[0], y1[1]), pk2(y1[2], y1[3])});
    }
    pin(ys[0]); pin(ys[1]);
    barl();
  }
  unsigned sq[8];
  sfor<8>([&](auto I) { ld4c<(I >> 2) * 1024 + (I & 3) * 64>(sq[I], ws + O_SS + (8 * T + 2 * p) * 1024, q * 256 + c * 4); });
  const char* zs = ws + O_XC + (size_t)(8 * T + w) * 18 * 1024;
  sfor<9>([&](auto I) { dmac(L + (9 + I) * 8192 + w * 1024, zs + I * 1024, vz); });
  st16c(ws + O_YT + (size_t)(2 * b + tf) * 4096 + p * 1024, vz, u32x4{pk2(ys[0][0], ys[0][1]), pk2(ys[0][2], ys[0][3]), pk2(ys[1][0], ys[1][1]), pk2(ys[1][2], ys[1][3])});
  const char* wd = w3 + 2304 * g + 2048 + 64 * s;
  const int rb = 8 * w + (lane >> 3), rd = 16 * w + (lane >> 2);
  const unsigned vb = rb * 9216 + (((lane & 7) ^ (((rb >> 1) & 3) << 1)) * 16), vd = rd * 9216 + (((lane & 3) ^ (((rd >> 2) & 1) << 1)) * 16);
  auto issue = [&](auto N) {
    constexpr int n = N; const unsigned dst = L + phys(n) * 8192 + w * 1024;
    if constexpr (n < 36) { if constexpr (n & 1) dma(dst, wa + (size_t)(n >> 1) * 32 * 9216, va); else dmac(dst, zs + (n >> 1) * 1024, vz); }
    else if constexpr (n < 45) dma(dst, wa + 256 + (size_t)(n - 36) * 64 * 9216, vb);
    else if constexpr (n < 54) dma(dst, wa + 384 + (size_t)(n - 45) * 64 * 9216, vb);
    else if constexpr (n < 58) dma(dst, wd + (size_t)(n - 54) * 128 * 9216, vd);
    else dma(dst, wd + (size_t)(tf ? 448 : 512) * 9216, vd);
  };
  const char* wk = w4 + (size_t)1327104 * g;
  const char* hb = ws + O_H + ((size_t)(8 * T + 2 * p) * 144 + 36 * g) * 1024;
  auto ks4 = [&](int j) { return j < 8 ? 8 * ((s + (j >> 1)) & 3) + 2 * (j & 1) : j < 16 ? 8 * ((s + j) & 3) + (j >> 2) * 2 : 2 * j; };
  unsigned ta[8], tz = L + 73728 + p * 2048 + vz, t4e, fv[4];
  u32x4 fc;
  ta[0] = L + row * 256 + (((4 * tf) ^ (row & 7)) * 32) + (c & 3) * 8;
  sfor<4>([&](auto I) { ta[I] = ta[0] ^ (I * 32); pin(ta[I]); }); pin(tz);
  bf16x8 Z[18][2], A[4], B[16][2];
  u32x2 Bs[2][2][2];
  f32x4 acc[9][2], ac4[9][2];
  auto rd_ = [&](auto I) {
    constexpr int i = I, wi = i >> 2, ph = phys(endidx(wi) - 1), hi = ph >= 9, so = (ph % 9) * 8192;
    if constexpr (i == 72) sfor<4>([&](auto K) { ta[K] = ((L + row * 128 + (((2 * tf) ^ ((row >> 1) & 3)) * 32) + (c & 3) * 8) ^ ((K & 1) * 32)) + (K >> 1) * 73728; pin(ta[K]); });
    if constexpr (i == 144) sfor<2>([&](auto K) { ta[K] = L + row * 64 + ((tf ^ ((row >> 2) & 1)) * 32) + (c & 3) * 8 + K * 73728; pin(ta[K]); });
    if constexpr (i == 162) { sfor<8>([&](auto K) { ta[K] = (L + tf * 73728 + row * 256 + (((row & 7) ^ (2 * g)) * 32) + (c & 3) * 8) ^ (K * 32); pin(ta[K]); }); t4e = L + L_SP + tf * 1024 + row * 32 + (c & 3) * 8; pin(t4e); }
    if constexpr (i < 72) {
      if constexpr ((i & 3) == 0) sfor<2>([&](auto K) { Z[wi][K] = wi < 16 ? ldsfrag(tz + so + K * 1024) : splitfrag(tz - lane * 8 + so + K * 1024); });
      A[i & 3] = trfrag(ta[i & 3] + so, 4096);
    } else if constexpr (i < 144) A[i & 3] = trfrag(ta[2 * hi + (i & 1)] + so + ((i >> 1) & 1) * 4096, 2048);
    else if constexpr (i < 162) A[i & 3] = trfrag(ta[hi] + so + (i & 3) * 2048, 1024);
    else { constexpr int j = (i - 162) / 9, u = (i - 162) % 9; if constexpr (u < 8) A[i & 3] = trfrag(ta[u] + ((3 + j) & 7) * 8192, 4096); else A[i & 3] = trfrag(t4e + (j & 7) * 2048, 512); }
  };
  waitv<vcnt(0)>(); bar();
  TS(11);
  sfor<4>(rd_);
  float ca[2], cb[2], sg[2], gp;
  unsigned pk[4];
  auto gl = [&](auto V) {
    constexpr int v = V, pr = v >> 4, t2 = v < 64 ? (v >> 3) & 1 : (v >> 2) & 1, tl = v < 64 ? 2 * pr + ((v >> 2) & 1) : 8;
    const float o = gelu(acc[tl][t2][v & 3], ca[t2], cb[t2], sg[t2]);
    if constexpr (v & 1) pk[(v >> 1) & 3] = pk2(gp, o); else gp = o;
    if constexpr (v < 64 && (v & 7) == 7) st16c(hb + (t2 * 144 + 8 * s + (pr < 2 ? 2 * tf + pr : 2 * pr + tf)) * 1024, vz, u32x4{pk[0], pk[1], pk[2], pk[3]});
    if constexpr (v >= 64 && (v & 3) == 3) st8c(hb + (t2 * 144 + 32 + s) * 1024 + tf * 512, lane * 8, u32x2{pk[2 * t2], pk[2 * t2 + 1]});
  };
  const unsigned f3o = (T * 32 + g * 4 + (lane & 3)) * 4;
  auto step = [&](auto I) {
    constexpr int i = I, j = (i - 162) / 9, u = (i - 162) % 9;
    if constexpr (i < 162 ? (i + 4) % 8 == 0 : (u == 5 && j < 17)) {
      waitv<vcnt(i)>(); bar();
      if constexpr (i >= 156) { constexpr int jn = i < 162 ? 0 : j + 1; if constexpr (jn < 16) { pin(B[jn][0]); pin(B[jn][1]); } else sfor<4>([&](auto K) { pin(Bs[jn - 16][K >> 1][K & 1]); }); }
      if constexpr (i == 36) { if (tid == 0) st4c(fl + FC, b * 4, tag); }
      sfor<4>([&](auto E) {
        if constexpr (PUB[E] == i) { if (tid == 0) st4c(fl + F3 + E * 2048, fid, tag); }
        if constexpr (EXM[E] == i) { pin(fv[E]); if (!allq(fv[E], tag)) spin<unsigned>(fl + F3 + E * 2048, f3o, tag); }
      });
      if constexpr (EXM[4] == i) { pin(fc); if (!allq(fc, tag)) spin<u32x4>(fl + FC, vz, tag); }
    }
    const f32x4 z4 = {};
    if constexpr (i < 162) {
      constexpr int ks = i < 72 ? i >> 2 : i < 108 ? (i - 72) >> 1 : i < 144 ? (i - 108) >> 1 : i - 144;
      constexpr int tl = i < 72 ? i & 3 : i < 108 ? 4 + (i & 1) : i < 144 ? 6 + (i & 1) : 8;
      sfor<2>([&](auto K) { acc[tl][K] = mfma(A[i & 3], Z[ks][K], ks == 0 ? z4 : acc[tl][K]); });
    } else sfor<2>([&](auto K) {
      bf16x8 bf;
      if constexpr (j < 16) bf = B[j][K]; else bf = __builtin_bit_cast(bf16x8, __builtin_shufflevector(Bs[j - 16][K][0], Bs[j - 16][K][1], 0, 1, 2, 3));
      ac4[u][K] = mfma(A[i & 3], bf, j == 0 ? z4 : ac4[u][K]);
    });
    if constexpr (i + 4 < 324) rd_(IC<i + 4>{});
    if constexpr (i >= 4 && i < 162) { constexpr int G = (i + 4) / 8, k = (i + 4) % 8; if constexpr ((k & 1) && lim3(G - 1) + (k >> 1) < lim3(G)) issue(IC<lim3(G - 1) + (k >> 1)>{}); }
    sfor<36>([&](auto N) {
      constexpr int jj = N >> 1, k = N & 1;
      if constexpr (wIt(jj, k) == i) dma(L + (((3 + jj) & 7) + 9 * k) * 8192 + w * 1024, wk + 256 * s + (ks4(jj) + k) * 36864, vo);
      if constexpr (bIt(jj, k) == i) {
        const char* hp = hb + ((k ^ tf) * 144 + ks4(jj) + tf) * 1024;
        if constexpr (jj < 16) ld16c(B[jj][k], hp, vz); else { ld8c<0>(Bs[jj - 16][k][0], hp, lane * 8); ld8c<512>(Bs[jj - 16][k][1], hp, lane * 8); }
      }
    });
    sfor<5>([&](auto E) {
      if constexpr (eIt(E) == i) dma(L + L_SP + (E & 1) * 8192 + w * 1024, wk + 1024 + 32 * s + (ks4(E < 4 ? 4 * E + (w >> 1) : 16 + (p >> 1)) + (w & 1)) * 36864, ve);
      if constexpr (LDF[E] == i) { if constexpr (E < 4) ld4c<0>(fv[E], fl + F3 + E * 2048, f3o); else ld16c(fc, fl + FC, vz); }
    });
    if constexpr (i == 36) TS(12);
    if constexpr (i == 72) TS(13);
    if constexpr (i == 108) TS(14);
    if constexpr (i == 144) TS(15);
    if constexpr (i == 162) TS(16);
    if constexpr (i == 216) TS(17);
    if constexpr (i == 270) TS(18);
    if constexpr (i == 60) sfor<2>([&](auto J) {
      float y = 0;
      sfor<4>([&](auto K) { pin(sq[4 * J + K]); y += __builtin_bit_cast(float, sq[4 * J + K]); });
      const float vr = sumq(y) * (1.f / D_) + EPS, r = __builtin_amdgcn_rsqf(vr); sg[J] = vr * r; ca[J] = -GK * r; cb[J] = -GK * GC * r * r * r;
    });
    if constexpr (gV(i) >= 0) gl(IC<gV(i)>{});
    SB();
  };
  sfor<162>(step);
  sfor<162>([&](auto I) { step(IC<162 + I>{}); });
  TS(19);
  bar();
  sfor<9>([&](auto U) { *(LDS3 f32x4*)(L + w * 9216 + U * 1024 + vz) = ac4[U][1]; });
  barl();
  f32x4 z[9];
  sfor<9>([&](auto U) { z[U] = ac4[U][0] + *(LDS3 f32x4*)(L + (w ^ 4) * 9216 + U * 1024 + vz); });
  sfor<3>([&](auto R) { constexpr int r = R + 1; st16c(ws + O_P + (size_t)(((b ^ r) * 3 + R) * 8 + w) * 1024, vz, u32x4{pk2(z[2 * r][0], z[2 * r][1]), pk2(z[2 * r][2], z[2 * r][3]), pk2(z[2 * r + 1][0], z[2 * r + 1][1]), pk2(z[2 * r + 1][2], z[2 * r + 1][3])}); });
  const bool own = p == g;
  if (!own) st8c(ws + O_PE + (size_t)(((b ^ p ^ g) * 3 + (p ^ g) - 1) * 2 + tf) * 512, lane * 8, u32x2{pk2(z[8][0], z[8][1]), pk2(z[8][2], z[8][3])});
  const int fr = 8 * T + 2 * p + tf, t = 16 * fr + c, n0 = 128 * s + 32 * g + 4 * q, ne = 512 + 16 * s + 4 * q;
  unsigned yv[12], mu; u32x2 xa[3], st, ce, pe[3]; u32x4 cm, pm[3];
  sfor<12>([&](auto I) { ld4c<0>(yv[I], ws + O_YT + ((I & 3) + (I == 4 || I == 5 || I == 6 || I == 7 ? 16 : 0)) * 4096, (I < 8 ? n0 : ne) * 4096 + (t >> 1) * 4); });
  ld8<0>(xa[0], x, t * 1152 + n0 * 2); ld8<32>(xa[1], x, t * 1152 + n0 * 2); ld8<0>(xa[2], x, t * 1152 + ne * 2);
  ld8c<0>(st, ws + O_XST, t * 8); ld4c<0>(mu, ws + O_MU, t * 4);
  ld16c(cm, ws + O_XC + (size_t)(fr * 18 + 4 * s + g) * 1024, vz); ld8c<0>(ce, ws + O_XC + (size_t)(fr * 36 + 32 + s) * 512, lane * 8);
  waitv<19>(); bar();
  TS(20);
  if (w == 0) { if (lane == 0) st4c(fl + F4, fid, tag); spin<unsigned>(fl + F4, (T * 32 + (lane & 3) * 4 + s) * 4, tag); }
  bar();
  TS(21);
  sfor<3>([&](auto R) { ld16c(pm[R], ws + O_P + (size_t)((b * 3 + R) * 8 + w) * 1024, vz); ld8c<0>(pe[R], ws + O_PE + (size_t)((b * 3 + R) * 2 + tf) * 512, lane * 8); });
  waitv<0>();
  TS(22);
  sfor<12>([&](auto I) { pin(yv[I]); }); sfor<3>([&](auto I) { pin(xa[I]); pin(pm[I]); pin(pe[I]); }); pin(st); pin(mu); pin(cm); pin(ce);
  const f32x2 sf = __builtin_bit_cast(f32x2, st);
  auto fin = [&](auto H) {
    float r[4];
    sfor<4>([&](auto E) {
      constexpr int e = E, k = 2 * H + (e >> 1);
      auto X = [&](unsigned u) { return (e & 1) ? bhi(u) : blo(u); };
      float ob = __builtin_bit_cast(float, mu) + z[H < 2 ? H : 8][e];
      if constexpr (H < 2) ob += (X(cm[k]) + X(pm[0][k])) + (X(pm[1][k]) + X(pm[2][k])); else ob += (X(ce[k - 4]) + X(pe[0][k - 4])) + (X(pe[1][k - 4]) + X(pe[2][k - 4]));
      const float xf = X(xa[H][e >> 1]), y = (c & 1) ? bhi(yv[4 * H + e]) : blo(yv[4 * H + e]);
      r[e] = xf + (xf - sf[0]) * sf[1] * y * (ob * __builtin_amdgcn_rcpf(1.f + __builtin_amdgcn_exp2f(-1.44269504f * ob)));
    });
    *(u32x2*)(out + t * 1152 + (H < 2 ? n0 + 16 * H : ne) * 2) = u32x2{pk2(r[0], r[1]), pk2(r[2], r[3])};
  };
  fin(IC<0>{}); fin(IC<1>{});
  if (own) fin(IC<2>{});
  TS(23);
  TSEND;
}

extern "C" void kernel_launch(void* const* d_in, const int* in_sizes, int n_in, void* d_out, int out_size, void* d_ws, size_t ws_size, hipStream_t stream) {
  k_all<<<256, 512, 0, stream>>>((const char*)d_in[0], (const char*)d_in[1], (const char*)d_in[2], (const char*)d_in[3], (const char*)d_in[4], (const unsigned*)d_in[5],
                                 (const char*)d_in[7], (const char*)d_in[8], (const char*)d_in[9], (const char*)d_in[10], (char*)d_ws, (char*)d_out);
}
```

```cpp
#include <hip/hip_runtime.h>
#include <hip/hip_bf16.h>
#include <utility>

typedef __attribute__((ext_vector_type(2))) __bf16 bf16x2;
typedef __attribute__((ext_vector_type(4))) __bf16 bf16x4;
typedef __attribute__((ext_vector_type(8))) __bf16 bf16x8;
typedef __attribute__((ext_vector_type(2))) float f32x2;
typedef __attribute__((ext_vector_type(4))) float f32x4;
typedef __attribute__((ext_vector_type(2))) unsigned u32x2;
typedef __attribute__((ext_vector_type(4))) unsigned u32x4;
#define LDS3 __attribute__((address_space(3)))
#define DEV __device__ __forceinline__
#define SB() __builtin_amdgcn_sched_barrier(0)

constexpr int S_ = 2048, D_ = 576, F_ = 4608;
constexpr float EPS = 1e-5f;
constexpr float GK = 2.302208198f;
constexpr float GC = 0.044715f;
constexpr size_t SZ_ACT = (size_t)S_ * D_ * 2;
constexpr size_t O_HS = 0, O_XC = O_HS + SZ_ACT, O_KVT = O_XC + SZ_ACT, O_YT = O_KVT + SZ_ACT,
                 O_P = O_YT + SZ_ACT, O_H = O_P + 4 * SZ_ACT, O_XST = O_H + 8 * SZ_ACT,
                 O_MU = O_XST + 16384, O_SS = O_MU + 8192, O_RM = O_SS + 131072, O_E = O_RM + 2048, O_F = O_E + 1024, O_PE = O_P + 3 * SZ_ACT;
constexpr int FK = 0, FC = 1024, F2 = 2048, F3 = 4096, F1 = F3 + 4 * 2048;

template <class F, int... I> DEV void sfor_(F&& f, std::integer_sequence<int, I...>) { (f(std::integral_constant<int, I>{}), ...); }
template <int N, class F> DEV void sfor(F&& f) { sfor_(f, std::make_integer_sequence<int, N>{}); }

DEV void dma(unsigned lds, const char* g, unsigned voff) {
  asm volatile("s_mov_b32 m0, %0\n\ts_nop 0\n\tglobal_load_lds_dwordx4 %1, %2" ::"s"(lds), "v"(voff), "s"(g) : "memory");
}
template <int N> DEV void waitv() { asm volatile("s_waitcnt vmcnt(%0)" ::"n"(N) : "memory"); }
DEV void bar() { asm volatile("s_barrier" ::: "memory"); }
DEV void barl() { asm volatile("s_waitcnt lgkmcnt(0)\n\ts_barrier" ::: "memory"); }
template <int O> DEV void ld8(u32x2& d, const char* g, unsigned voff) { asm volatile("global_load_dwordx2 %0, %1, %2 offset:%3" : "=v"(d) : "v"(voff), "s"(g), "n"(O) : "memory"); }
template <int O> DEV void ld16(u32x4& d, const char* g, unsigned voff) { asm volatile("global_load_dwordx4 %0, %1, %2 offset:%3" : "=v"(d) : "v"(voff), "s"(g), "n"(O) : "memory"); }
template <int N, class T> DEV void syncb(T& a, T& b) { asm volatile("s_waitcnt vmcnt(%2)\n\ts_barrier" : "+v"(a), "+v"(b) : "n"(N) : "memory"); }
#define TS(i)
#define TSDECL
#define TSEND
DEV void dmac(unsigned lds, const char* g, unsigned voff) {
  asm volatile("s_mov_b32 m0, %0\n\ts_nop 0\n\tglobal_load_lds_dwordx4 %1, %2 sc1" ::"s"(lds), "v"(voff), "s"(g) : "memory");
}
template <int O> DEV void ld4(unsigned& d, const char* g, unsigned voff) { asm volatile("global_load_dword %0, %1, %2 offset:%3" : "=v"(d) : "v"(voff), "s"(g), "n"(O) : "memory"); }
template <int O> DEV void ld4c(unsigned& d, const char* g, unsigned voff) { asm volatile("global_load_dword %0, %1, %2 offset:%3 sc1" : "=v"(d) : "v"(voff), "s"(g), "n"(O) : "memory"); }
template <int O> DEV void ld8c(u32x2& d, const char* g, unsigned voff) { asm volatile("global_load_dwordx2 %0, %1, %2 offset:%3 sc1" : "=v"(d) : "v"(voff), "s"(g), "n"(O) : "memory"); }
template <class T> DEV void ld16c(T& d, const char* g, unsigned voff) { asm volatile("global_load_dwordx4 %0, %1, %2 sc1" : "=v"(d) : "v"(voff), "s"(g) : "memory"); }
DEV void st4c(const char* g, unsigned voff, unsigned d) { asm volatile("global_store_dword %0, %1, %2 sc1" ::"v"(voff), "v"(d), "s"(g) : "memory"); }
DEV void st8c(const char* g, unsigned voff, u32x2 d) { asm volatile("global_store_dwordx2 %0, %1, %2 sc1" ::"v"(voff), "v"(d), "s"(g) : "memory"); }
template <int O = 0> DEV void st16c(const char* g, unsigned voff, u32x4 d) { asm volatile("global_store_dwordx4 %0, %1, %2 offset:%3 sc1\n\ts_nop 1" ::"v"(voff), "v"(d), "s"(g), "n"(O) : "memory"); }
DEV bool allq(unsigned v, unsigned tag) { return __builtin_amdgcn_ballot_w64(v != tag) == 0; }
DEV bool allq(u32x4 v, unsigned tag) { return __builtin_amdgcn_ballot_w64(((v[0] ^ tag) | (v[1] ^ tag) | (v[2] ^ tag) | (v[3] ^ tag)) != 0) == 0; }
template <class T> DEV void spin(const char* g, unsigned voff, unsigned tag) {
#pragma unroll 1
  for (int it = 0; it < (1 << 17); ++it) {
    T v;
    if constexpr (sizeof(T) == 4) asm volatile("global_load_dword %0, %1, %2 sc1\n\ts_waitcnt vmcnt(0)" : "=&v"(v) : "v"(voff), "s"(g) : "memory");
    else asm volatile("global_load_dwordx4 %0, %1, %2 sc1\n\ts_waitcnt vmcnt(0)" : "=&v"(v) : "v"(voff), "s"(g) : "memory");
    if (allq(v, tag)) break;
    __builtin_amdgcn_s_sleep(2);
  }
}
template <class T> DEV void pin(T& d) { asm volatile("" : "+v"(d)); }

DEV bf16x8 trfrag(unsigned a, int second) {
  bf16x4 x = __builtin_amdgcn_ds_read_tr16_b64_v4bf16((LDS3 bf16x4*)a);
  bf16x4 y = __builtin_amdgcn_ds_read_tr16_b64_v4bf16((LDS3 bf16x4*)(a + second));
  return __builtin_shufflevector(x, y, 0, 1, 2, 3, 4, 5, 6, 7);
}
DEV bf16x8 ldsfrag(unsigned a) { return *(LDS3 bf16x8*)a; }
DEV f32x4 mfma(bf16x8 a, bf16x8 b, f32x4 c) { return __builtin_amdgcn_mfma_f32_16x16x32_bf16(a, b, c, 0, 0, 0); }
DEV unsigned pk2(float a, float b) { f32x2 v = {a, b}; return __builtin_bit_cast(unsigned, __builtin_convertvector(v, bf16x2)); }
DEV float blo(unsigned u) { return __builtin_bit_cast(float, u << 16); }
DEV float bhi(unsigned u) { return __builtin_bit_cast(float, u & 0xffff0000u); }
DEV float gelu(float u, float ca, float cb, float sg) {
  float t = u * u; float p = __builtin_fmaf(t, cb, ca); float e = __builtin_amdgcn_exp2f(u * p);
  float o = u * __builtin_amdgcn_rcpf(__builtin_fmaf(e, sg, sg)); pin(o); return o;
}
template <int C> DEV float dpp(float v) { return __builtin_bit_cast(float, __builtin_amdgcn_update_dpp(0, __builtin_bit_cast(int, v), C, 0xf, 0xf, true)); }
DEV float sum16(float v) { v += dpp<0x128>(v); v += dpp<0x124>(v); v += dpp<0x122>(v); v += dpp<0x121>(v); return v; }
DEV float sum8(float v) { v += dpp<0xB1>(v); v += dpp<0x4E>(v); v += dpp<0x141>(v); return v; }
DEV float dsum(unsigned u, float a) { return __builtin_amdgcn_fdot2_f32_bf16(__builtin_bit_cast(bf16x2, u), __builtin_bit_cast(bf16x2, 0x3f803f80u), a, false); }
DEV float sumq(float v) { v += __shfl_xor(v, 16); v += __shfl_xor(v, 32); return v; }

DEV bf16x8 splitfrag(unsigned a) { const u32x2 x = *(LDS3 u32x2*)a, y = *(LDS3 u32x2*)(a + 512); return __builtin_bit_cast(bf16x8, u32x4{x[0], x[1], y[0], y[1]}); }
template <int I> using IC = std::integral_constant<int, I>;
#define TX(i)

constexpr int L_CV = 131072, CV_SZ = 8736, CV_U = 512, CV_H = 4640, L_EXT = 100352, L_ACT = 118784, L_HS = 73728, L_EX2 = 110592, L_RM = 129024, L_RB = 98304, L_SP = 147456, L_JK = 163584;
DEV void pf(unsigned L, const char* g, unsigned voff) { asm volatile("s_mov_b32 m0, %0\n\ts_nop 0\n\tglobal_load_lds_dword %1, %2" ::"s"(L + L_JK), "v"(voff), "s"(g) : "memory"); }
DEV bf16x8 toep(unsigned a, unsigned sh) {
  const LDS3 unsigned* hp = (const LDS3 unsigned*)a;
  const unsigned d0 = hp[0], d1 = hp[1], d2 = hp[2], d3 = hp[3], d4 = hp[4];
  return __builtin_bit_cast(bf16x8, u32x4{__builtin_amdgcn_alignbyte(d1, d0, sh), __builtin_amdgcn_alignbyte(d2, d1, sh), __builtin_amdgcn_alignbyte(d3, d2, sh), __builtin_amdgcn_alignbyte(d4, d3, sh)});
}
constexpr int imin(int a, int b) { return a < b ? a : b; }
constexpr int imax(int a, int b) { return a > b ? a : b; }
constexpr int endidx(int wi) { return wi < 0 ? 0 : wi < 18 ? 2 * wi + 2 : wi > 40 ? 59 : 19 + wi; }
constexpr int lim3(int G) { return imin(59, 18 + endidx(2 * G - 2)); }
constexpr int phys(int n) { return ((n % 18) & 1) ? (n % 18) >> 1 : 9 + ((n % 18) >> 1); }
constexpr int slotIt(int n) { int G = 1; while (lim3(G) <= n) ++G; return 8 * G - 3 + 2 * (n - lim3(G - 1)); }
constexpr int PUB[4] = {124, 176, 212, 230}, LDF[5] = {143, 190, 226, 250, 216}, EXM[5] = {156, 203, 239, 266, 239};
constexpr int CODE = 56064, FIN = 284, CLR = 42, PF4 = -1;
constexpr int gI(int v) { return v < 32 ? 74 + v * 4 / 3 : v < 48 ? 118 + (v - 32) * 11 / 4 : 162 + (v - 48) * 5 / 2; }
constexpr int gV(int n) { for (int v = 0; v < 72; ++v) if (gI(v) == n) return v; return -1; }
constexpr int wIt(int j, int k) { return j < 5 ? 109 + 8 * j + 2 * k : 162 + 9 * (j - 5) + 2 + k; }
constexpr int bIt(int j, int k) { return j < 2 ? PUB[0] + 2 + 2 * j + k : j < 4 ? EXM[0] - 3 + 2 * j + k : 162 + 9 * (j - 4) + k; }
constexpr int eIt(int e) { return e < 2 ? 101 + 30 * e : 162 + 9 * (4 * e - 4) + 6; }
constexpr int nops(int n) {
  if (n < 0) return n == -2 ? 8 : 1;
  int c = 0;
  for (int i = 18; i < 59; ++i) c += slotIt(i) == n;
  for (int j = 0; j < 18; ++j) for (int k = 0; k < 2; ++k) c += (wIt(j, k) == n) + (bIt(j, k) == n) * (j < 16 ? 1 : 2);
  for (int e = 0; e < 5; ++e) c += (eIt(e) == n) + (LDF[e] == n);
  c += (n >= FIN && n < FIN + 18) + (n == CLR || n == CLR + 4 || n == CLR + 8);
  const int v = gV(n);
  return c + (v >= 0 && (v < 64 ? (v & 7) == 7 : (v & 3) == 3));
}
constexpr int need4(int j) { return imax(imax(wIt(j, 1), bIt(j, 1)), eIt(j / 4)); }
constexpr int vcnt(int n) {
  int l = 0;
  if (n < 162) { const int e = endidx(2 * ((n + 4) / 8) + 1); l = e <= 18 ? -12 + e / 2 : slotIt(e - 1); if (n == 156) l = imax(l, need4(0)); }
  else l = need4((n - 162) / 9 + 1);
  for (int e = 0; e < 5; ++e) { if (e < 4 && PUB[e] == n) l = imax(l, gI(e < 3 ? 31 + 16 * e : 71)); if (EXM[e] == n) l = imax(l, LDF[e]); }
  int c = 0;
  for (int m = l + 1; m < n; ++m) c += nops(m);
  return c;
}

__global__ __launch_bounds__(512, 2) void k_all(const char* __restrict__ x, const char* __restrict__ kk, const char* __restrict__ vv, const char* __restrict__ shk,
    const char* __restrict__ ssk, const unsigned* __restrict__ ssd, const char* __restrict__ w1, const char* __restrict__ w2, const char* __restrict__ w3,
    const char* __restrict__ w4, char* __restrict__ ws, char* __restrict__ out) {
  __shared__ __attribute__((aligned(1024))) char smem[163840];
  const unsigned L = (unsigned)(size_t)(LDS3 char*)smem;
  const int b = blockIdx.x, tid = threadIdx.x, lane = tid & 63, w = __builtin_amdgcn_readfirstlane(tid >> 6);
  const int g = b & 3, T = ((b >> 2) & 1) * 8 + (b >> 5), s = (b >> 3) & 3, T32 = 4 * T + g, g2 = s, xi = b >> 3;
  const int q = lane >> 4, c = lane & 15, tf = w >> 2, p = w & 3, row = 4 * q + (c >> 2);
  const char* fl = ws + O_F;
  const int fid = (T * 32 + g * 4 + s) * 4, ch3 = 512 + (b >> 2), chA = 8 * (b >> 2) + g;
  TSDECL;
  if (w == 7) { const char* pc; asm volatile("s_getpc_b64 %0" : "=s"(pc)); pf(L, pc, min(((xi & 7) * 64 + lane) * 128, CODE)); }
  if (w == 5) sfor<3>([&](auto I) { pf(L, w1 + 20736 * xi, min(lane + 64 * I, 161) * 128); });
  unsigned tag;
  const unsigned vz = lane * 16;
  const bool ext = p == tf;
  const int rr = 4 * w + q;
  const unsigned vo = rr * 1152 + ((c ^ ((rr & 7) << 1)) * 16), ve = (lane >> 1) * 1152 + (lane & 1) * 16;
  const unsigned t0 = L + row * 256 + (((2 * p) ^ (row & 7)) * 32) + (c & 3) * 8, t1 = t0 ^ 32, te = L + L_EXT + row * 32 + (c & 3) * 8;
  const char* wm2 = w2 + 256 * g2;
  {
    const char* wm = w1 + 288 * g2 + 32 * (g2 & 1);
    const char* we = w1 + 288 * g2 + ((g2 & 1) ? 0 : 256);
    u32x2 ar[19]; u32x4 sk[2];
    const int kb = 36 * g2 + 12 * p, kd = kb + min(c, 11), kt = 32 * T32 + 8 * q;
    const int rmr = (q < 2 || b >= 32) ? 2 * b + (q & 1) : 510 + 2 * b + q;
    f32x2 xs = {}; int xr = -1;
    if (w < 4) {
      const int r = tid >> 3, e = tid & 7, srow = 32 * T32 + r + ((g2 & 1) ? 1 : -1) * (1 + (g2 >> 1));
      const bool ok = srow >= 0 && srow < S_;
      u32x4 xv[9];
      { const unsigned xo = (unsigned)(ok ? srow : 0) * 1152 + e * 16; sfor<9>([&](auto I) { ld16<I * 128>(xv[I], x, xo); }); }
      ld4c<0>(tag, ws + O_E, 0u);
      bar(); waitv<0>();
      TS(1);
      sfor<9>([&](auto I) { pin(xv[I]); }); pin(tag);
      float sm[4] = {};
      sfor<36>([&](auto I) { sm[I & 3] = dsum(xv[I >> 2][I & 3], sm[I & 3]); });
      const float m = sum8((sm[0] + sm[1]) + (sm[2] + sm[3])) * (1.f / D_);
      f32x2 f[36], v2[2] = {};
      sfor<36>([&](auto I) { const unsigned u = xv[I >> 2][I & 3]; f[I] = f32x2{blo(u), bhi(u)} - m; v2[I & 1] += f[I] * f[I]; });
      const float v = sum8((v2[0][0] + v2[0][1]) + (v2[1][0] + v2[1][1])) * (1.f / D_);
      const float rs = __builtin_amdgcn_rsqf(v + EPS);
      const float sc = ok ? rs * __builtin_amdgcn_rsqf(v * rs * rs + EPS) : 0.f;
      const unsigned za = L + L_ACT + r * 1184 + (e >> 2) * 64 + (e & 1) * 32 + (e & 2) * 4;
      sfor<18>([&](auto I) { const f32x2 y0 = f[2 * I] * sc, y1 = f[2 * I + 1] * sc; *(LDS3 u32x2*)(za + (I >> 1) * 128 + (I & 1) * 16) = u32x2{pk2(y0[0], y0[1]), pk2(y1[0], y1[1])}; });
      xs = f32x2{m, rs}; xr = g2 < 2 && ok && e == 0 ? srow * 8 : -1;
    } else {
      ld4c<0>(tag, ws + O_E, 0u);
      bar();
      sfor<12>([&](auto N) {
        dma(L + N * 8192 + p * 1024, wm + (size_t)N * 32 * 1152, vo - 18432); dma(L + N * 8192 + w * 1024, wm + (size_t)N * 32 * 1152, vo);
        if (p == (N & 3)) dma(L + L_EXT + N * 1024, we + (size_t)N * 32 * 1152, ve);
      });
      TS(13);
      waitv<24>(); pin(tag);
    }
    if (tf) {
      const bool kw = w < 7;
      const char *b0 = kw ? kk : w2, *b1 = kw ? vv : w2;
      const int o0 = kw ? (kt - 3) * 1152 + kd * 8 : rmr * 1152 + c * 8, s0 = kw ? 1152 : 128, m0 = kw ? 1 << 30 : o0 + 1024;
      const int o1 = kw ? o0 + 3456 : 20736 * xi + lane * 128, s1 = kw ? 1152 : 8192, m1 = kw ? 1 << 30 : 20736 * xi + 161 * 128;
      sfor<11>([&](auto I) { ld8<0>(ar[I], b0, min(max(o0 + I * s0, 0), m0)); });
      sfor<8>([&](auto I) { ld8<0>(ar[11 + I], b1, min(o1 + I * s1, m1)); });
      ld16<0>(sk[0], shk, min(kd, 143) * 32); ld16<16>(sk[1], shk, min(kd, 143) * 32);
      TS(12);
    }
    tag += 1;
    u32x4 kvo[4] = {};
    f32x4 a0 = {}, a1 = {}, ae = {};
    const unsigned bb = L + L_ACT + (16 * tf + c) * 1184 + q * 16;
    TS(2);
    sfor<6>([&](auto J) {
      constexpr int j = J;
      waitv<j == 0 ? 41 : j == 1 ? 34 : j == 2 ? 30 : j == 3 ? 27 : j == 4 ? 6 : 3>();
      barl();
      if constexpr (j == 0) TS(15); if constexpr (j == 3) TS(17); if constexpr (j == 5) TS(18);
      bf16x8 B[3], A0[3], A1[3];
      sfor<3>([&](auto I) { constexpr int ks = 3 * j + I, so = (ks % 12) * 8192; B[I] = ldsfrag(bb + ks * 64); A0[I] = trfrag(t0 + so, 4096); A1[I] = trfrag(t1 + so, 4096); });
      SB();
      if constexpr (j == 1 || j == 2) sfor<3>([&](auto I) { constexpr int n = 3 * (j - 1) + I; dma(L + n * 8192 + w * 1024, wm + (size_t)(12 + n) * 32 * 1152, vo); if (w == I) dma(L + L_EXT + (12 + n) * 1024, we + (size_t)(12 + n) * 32 * 1152, ve); });
      if constexpr (j == 3 || j == 5) sfor<3>([&](auto I) { constexpr int n = 3 * (j >> 2) + I; dma(L + ((n + 6) % 12) * 8192 + w * 1024, wm2 + (size_t)n * 32 * 1152, vo); });
      SB();
      if (ext) { bf16x8 E[3]; sfor<3>([&](auto I) { E[I] = trfrag(te + (3 * j + I) * 1024, 512); }); SB(); sfor<3>([&](auto I) { ae = mfma(E[I], B[I], ae); }); }
      sfor<3>([&](auto I) { a0 = mfma(A0[I], B[I], a0); a1 = mfma(A1[I], B[I], a1); });
      if constexpr (j == 3) { if (tf) {
        waitv<9>();
        TS(21);
        sfor<19>([&](auto I) { pin(ar[I]); }); pin(sk[0]); pin(sk[1]);
        if (w < 7) {
          const int l2 = min(lane, 47), sl = ((l2 & 3) * 16 + (l2 >> 2)) * 4;
          if (kt == 0) ar[0] = ar[1] = ar[2] = u32x2{0, 0};
          sfor<4>([&](auto H) {
            auto X = [&](const u32x2& u) { const unsigned y = u[H >> 1]; return (H & 1) ? bhi(y) : blo(y); };
            const unsigned s0 = sk[H >> 1][2 * (H & 1)], s1 = sk[H >> 1][2 * (H & 1) + 1];
            float r0[8];
            sfor<8>([&](auto I) { r0[I] = (X(ar[I + 3]) * blo(s0) + X(ar[I + 2]) * bhi(s0) + X(ar[I + 1]) * blo(s1) + X(ar[I]) * bhi(s1)) * X(ar[11 + I]); pin(r0[I]); });
            sfor<4>([&](auto I) { kvo[H][(int)I] = (unsigned)__builtin_amdgcn_ds_bpermute(sl, (int)pk2(r0[7 - 2 * I], r0[6 - 2 * I])); });
          });
        } else {
          float z = 0;
          sfor<18>([&](auto I) { z = dsum(ar[I >> 1][I & 1], z); });
          z = sum16(z) * (1.f / D_);
          kvo[0][0] = pk2(z, __shfl_xor(z, 16));
        }
      } }
    });
    TS(3);
    if (tf) {
      if (w < 7) sfor<4>([&](auto H) { st16c<0>(ws + O_KVT + H * 9216 + T32 * 36864 + kb * 64, min(lane, 47) * 16, kvo[H]); });
      else if ((lane & 31) == 0) { const int k = rmr & 31; st4c(ws + O_RM, ((rmr >> 5) * 32 + ((k >> 2) & 3) * 8 + (k >> 4) * 4 + (k & 3)) * 2, kvo[0][0]); }
    }
    auto G = [&](float u) { return gelu(u, -GK, -GK * GC, 1.f); };
    const char* hs = ws + O_HS + (size_t)(2 * T32 + tf) * 18 * 1024;
    st16c(hs + (((9 * g2 + 1) >> 1) + p) * 1024, vz, u32x4{pk2(G(a0[0]), G(a0[1])), pk2(G(a0[2]), G(a0[3])), pk2(G(a1[0]), G(a1[1])), pk2(G(a1[2]), G(a1[3]))});
    if (ext) st8c(hs + ((g2 & 1) ? 9 * g2 : 9 * g2 + 8) * 512, lane * 8, u32x2{pk2(G(ae[0]), G(ae[1])), pk2(G(ae[2]), G(ae[3]))});
    if (xr >= 0) st8c(ws + O_XST, xr, __builtin_bit_cast(u32x2, xs));
    sfor<3>([&](auto I) { dma(L + I * 8192 + w * 1024, wm2 + (size_t)(6 + I) * 32 * 1152, vo); });
    waitv<3>(); bar();
    if (tid == 0) st4c(fl + FK, b * 4, tag);
    TS(4);
  }
  const char* wa = w3 + 2304 * g + 512 * s;
  const char* wk = w4 + (size_t)1327104 * g;
  const char* zs = ws + O_XC + (size_t)(8 * T + w) * 18 * 1024;
  const unsigned f3o = (T * 32 + g * 4 + (lane & 3)) * 4;
  const unsigned va = rr * 9216 + ((c ^ ((rr & 7) << 1)) * 16);
  {
    const int t = 32 * T32 + 16 * tf + c;
    sfor<3>([&](auto I) { dma(L + (3 + I) * 8192 + w * 1024, wm2 + (size_t)(9 + I) * 32 * 1152, vo); });
    sfor<3>([&](auto I) { const int n = min(8 * I + w, 17); dma(L + L_EX2 + n * 1024, w2 + 1024 + 32 * g2 + (size_t)n * 32 * 1152, ve); });
    u32x2 st, xf; u32x4 xm;
    { const unsigned xo = (32 * T32 + 16 * tf + (lane >> 2)) * 1152; ld16<0>(xm, x + 256 * g2 + 64 * p, xo + (lane & 3) * 16); ld8<1024>(xf, x + 32 * g2, xo + (lane & 3) * 8); }
    bf16x8 A0[18], A1[18];
    auto rda = [&](auto I) { constexpr int so = (I < 9 ? (I + 6) % 12 : (I - 6) % 9) * 8192; A0[I] = trfrag(t0 + so, 4096); A1[I] = trfrag(t1 + so, 4096); };
    sfor<6>(rda);
    waitv<8>(); barl();
    sfor<3>([&](auto I) { dma(L + (6 + I) * 8192 + w * 1024, wm2 + (size_t)(12 + I) * 32 * 1152, vo); rda(IC<6 + I>{}); });
    if (w < 3 && lane < 34) *(LDS3 u32x4*)(L + L_CV + w * CV_SZ + (lane < 32 ? lane * 16 : 4096 + lane * 16)) = u32x4{0, 0, 0, 0};
    barl();
    sfor<3>([&](auto I) { dma(L + I * 8192 + w * 1024, wm2 + (size_t)(15 + I) * 32 * 1152, vo); });
    if (w == 0) spin<u32x4>(fl + FK, vz, tag);
    else waitv<0>();
    bar();
    TS(5);
    if (b == 0 && tid == 0) st4c(ws + O_E, 0u, tag);
    ld8c<0>(st, ws + O_XST, t * 8);
    sfor<5>([&](auto I) {
      const int n = min(8 * I + w, 37);
      dmac(L + (n < 36 ? L_HS : L_RM - 36864) + n * 1024, ws + (n < 36 ? O_HS + (size_t)36864 * T32 : O_RM - 36864) + n * 1024, vz);
    });
    bar();
    sfor<3>([&](auto I) {
      const int ch = I < 2 ? chA + 4 * I : ch3;
      dmac(L + L_CV + I * CV_SZ + (tf ? CV_H : CV_U) + p * 1024, tf ? ssk + (size_t)ch * 4096 + p * 1024 : ws + O_KVT + (size_t)16 * p * 36864 + (ch & 3) * 9216 + (ch >> 2) * 64, tf ? vz : (lane >> 2) * 36864 + (lane & 3) * 16);
    });
    sfor<9>([&](auto I) { rda(IC<9 + I>{}); });
    waitv<3>(); barl();
    if (w >= 2) { const int i = min(tid - 128, 323), i2 = i / 18; pf(L, w3 + 2304 * g, (18 * xi + i2) * 9216 + (i - 18 * i2) * 128); }
    TS(6);
    pin(xm); pin(xf);
    *(LDS3 u32x4*)(L + w * 2048 + vz) = xm; *(LDS3 u32x2*)(L + w * 2048 + 1024 + lane * 8) = xf;
    const u32x2 x0 = *(LDS3 u32x2*)(L + w * 2048 + c * 64 + q * 8), x1 = *(LDS3 u32x2*)(L + w * 2048 + c * 64 + q * 8 + 32), xe = *(LDS3 u32x2*)(L + w * 2048 + 1024 + c * 32 + q * 8);
    f32x4 a0 = {}, a1 = {}, ae = {}, am = {};
    const unsigned bb = L + L_HS + tf * 18432, tm = L + L_RM + q * 16;
    auto gm = [&](auto X) {
      bf16x8 B[18], M[18], E[18];
      auto rd = [&](auto I) { B[I] = (I == 4 || I == 13) ? splitfrag(bb + I * 1024 + lane * 8) : ldsfrag(bb + I * 1024 + vz); M[I] = ldsfrag(tm + I * 64); if constexpr (X) E[I] = trfrag(te + (L_EX2 - L_EXT) + I * 1024, 512); };
      sfor<3>(rd);
      sfor<18>([&](auto I) {
        if constexpr (I + 3 < 18) rd(IC<I + 3>{});
        a0 = mfma(A0[I], B[I], a0); a1 = mfma(A1[I], B[I], a1); am = mfma(M[I], B[I], am);
        if constexpr (X) ae = mfma(E[I], B[I], ae);
        SB();
      });
    };
    if (ext) gm(IC<1>{}); else gm(IC<0>{});
    TS(7);
    pin(st);
    const f32x2 sf = __builtin_bit_cast(f32x2, st); const float m = sf[0], rs = sf[1], mu = am[0];
    float ss = 0;
    auto E = [&](u32x2 xv, f32x4 a) {
      const float v0 = (blo(xv[0]) - m) * rs + a[0] - mu, v1 = (bhi(xv[0]) - m) * rs + a[1] - mu, v2 = (blo(xv[1]) - m) * rs + a[2] - mu, v3 = (bhi(xv[1]) - m) * rs + a[3] - mu;
      ss += (v0 * v0 + v1 * v1) + (v2 * v2 + v3 * v3);
      return u32x2{pk2(v0, v1), pk2(v2, v3)};
    };
    const char* xc = ws + O_XC + (size_t)(2 * T32 + tf) * 18 * 1024;
    const u32x2 o0 = E(x0, a0), o1 = E(x1, a1);
    st16c(xc + (4 * g2 + p) * 1024, vz, u32x4{o0[0], o0[1], o1[0], o1[1]});
    if (ext) st8c(xc + (32 + g2) * 512, lane * 8, E(xe, ae));
    ss = sumq(ss);
    if (lane < 16) { st4c(ws + O_SS + (2 * T32 + tf) * 1024 + (4 * g2 + p) * 64, lane * 4, __builtin_bit_cast(unsigned, ss)); if (g2 == 0 && p == 0) st4c(ws + O_MU, t * 4, __builtin_bit_cast(unsigned, mu)); }
    waitv<0>();
    if (w == 4 && lane < 3) {
      const unsigned d0 = ssd[chA >> 1], d1 = ssd[(chA >> 1) + 2], d3 = ssd[ch3 >> 1], dd = lane == 0 ? d0 : lane == 1 ? d1 : d3;
      LDS3 unsigned short* hp = (LDS3 unsigned short*)(L + L_CV + lane * CV_SZ + CV_H);
      *hp = (unsigned short)(pk2(blo(*hp) + (((lane < 2 ? chA : ch3) & 1) ? bhi(dd) : blo(dd)), 0.f) & 0xffff);
    }
    barl();
    if (tid == 0) st4c(fl + F2, fid, tag);
    TS(8);
  }
  f32x4 ys[2];
  {
    const int st = 9 + c - 8 * q;
    const unsigned sh = (st & 1) * 2, ho = L + L_CV + CV_H + (st >> 1) * 4, uo = L + L_CV + CV_U + (c - 1) * 32 + q * 16;
    f32x4 acc[8] = {}, c0 = {}, c1 = {};
    sfor<2>([&](auto S) {
      const unsigned ha = ho + tf * CV_SZ + (2 * p + S) * 64, ua = uo + tf * CV_SZ - (2 * p + S) * 64;
      bf16x8 A[8], B[8];
      A[0] = toep(ha, sh);
      sfor<8>([&](auto M) { B[M] = ldsfrag(ua + M * 512); });
      sfor<8>([&](auto M) {
        if constexpr (M < 4) A[M + 1] = toep(ha + (M + 1) * 512, sh); else if constexpr (M == 4) sfor<3>([&](auto I) { A[5 + I] = toep(ha + (5 + I) * 512, sh); });
        sfor<8 - M>([&](auto I) { acc[M + I] = mfma(A[M], B[I], acc[M + I]); });
        if constexpr (M < 6 - 3 * S) dma(L + (6 * S + M) * 8192 + w * 1024, wa + (size_t)(6 * S + M) * 32 * 9216, va);
        if constexpr (PF4 < 0 && S == 1 && M == 3) { if (w >= 2) pf(L, wk + xi * 41472, min(tid - 128, 323) * 128); }
      });
    });
    unsigned f2;
    ld4c<0>(f2, fl + F2 + T * 128, c * 4);
    { const unsigned ha = ho + 2 * CV_SZ + w * 64, ua = uo + 2 * CV_SZ - w * 64, za = L + L_CV + (lane & 31) * 16;
      sfor<8>([&](auto M) {
        const bf16x8 A = toep(ha + M * 512, sh);
        c1 = mfma(A, ldsfrag(M < 5 || 7 - g >= M ? ua + (7 - g - M) * 512 : za), c1);
        if constexpr (M < 4) c0 = mfma(A, ldsfrag(g >= M ? ua + (g - M) * 512 : za), c0);
      }); }
    TS(9);
    waitv<0>(); pin(f2);
    if (!allq(f2, tag)) spin<unsigned>(fl + F2 + T * 128, c * 4, tag);
    sfor<3>([&](auto I) { dmac(L + (9 + I) * 8192 + w * 1024, zs + I * 1024, vz); });
    barl();
    TS(10);
    const unsigned ra = L + L_RB + tf * 24576 + vz;
    sfor<4>([&](auto P) { if (p == P) {
      sfor<6>([&](auto I) { constexpr int r = 1 + (I >> 1), d = P ^ r; *(LDS3 f32x4*)(ra + (d * 3 + r - 1) * 2048 + (I & 1) * 1024) = acc[2 * d + (I & 1)]; });
      ys[0] = acc[2 * P]; ys[1] = acc[2 * P + 1];
    } });
    const unsigned rc = L + L_RB + 49152 + vz;
    if (w < 7) { *(LDS3 f32x4*)(rc + w * 2048) = c0; *(LDS3 f32x4*)(rc + w * 2048 + 1024) = c1; }
    barl();
    sfor<6>([&](auto I) { ys[I & 1] += *(LDS3 f32x4*)(ra + p * 6144 + I * 1024); });
    if (w == 7) sfor<7>([&](auto I) { c0 += *(LDS3 f32x4*)(rc + I * 2048); c1 += *(LDS3 f32x4*)(rc + I * 2048 + 1024); });
    pin(ys[0]); pin(ys[1]); pin(c0); pin(c1);
    waitv<1>();
    barl();
    if (w == 7) {
      st8c(ws + O_YT + (size_t)ch3 * 4096 + g * 512, c * 32 + q * 8, u32x2{pk2(c0[0], c0[1]), pk2(c0[2], c0[3])});
      st8c(ws + O_YT + (size_t)ch3 * 4096 + (7 - g) * 512, c * 32 + q * 8, u32x2{pk2(c1[0], c1[1]), pk2(c1[2], c1[3])});
    }
  }
  unsigned sq[8];
  sfor<6>([&](auto I) { dmac(L + (12 + I) * 8192 + w * 1024, zs + (3 + I) * 1024, vz); });
  TS(14);
  sfor<8>([&](auto I) { ld4c<(I >> 2) * 1024 + (I & 3) * 64>(sq[I], ws + O_SS + (8 * T + 2 * p) * 1024, q * 256 + c * 4); });
  st16c(ws + O_YT + (size_t)(chA + 4 * tf) * 4096 + p * 1024, c * 64 + q * 16, u32x4{pk2(ys[0][0], ys[0][1]), pk2(ys[0][2], ys[0][3]), pk2(ys[1][0], ys[1][1]), pk2(ys[1][2], ys[1][3])});
  const char* wd = w3 + 2304 * g + 2048 + 64 * s;
  const int rb = 8 * w + (lane >> 3), rd = 16 * w + (lane >> 2);
  const unsigned vb = rb * 9216 + (((lane & 7) ^ (((rb >> 1) & 3) << 1)) * 16), vd = rd * 9216 + (((lane & 3) ^ (((rd >> 2) & 1) << 1)) * 16);
  auto issue = [&](auto N) {
    constexpr int n = N; const unsigned dst = L + phys(n) * 8192 + w * 1024;
    if constexpr (n < 36) { if constexpr (n & 1) dma(dst, wa + (size_t)(n >> 1) * 32 * 9216, va); else dmac(dst, zs + (n >> 1) * 1024, vz); }
    else if constexpr (n < 45) dma(dst, wa + 256 + (size_t)(n - 36) * 64 * 9216, vb);
    else if constexpr (n < 54) dma(dst, wa + 384 + (size_t)(n - 45) * 64 * 9216, vb);
    else if constexpr (n < 58) dma(dst, wd + (size_t)(n - 54) * 128 * 9216, vd);
    else dma(dst, wd + (size_t)(tf ? 448 : 512) * 9216, vd);
  };
  const char* hb = ws + O_H + ((size_t)(8 * T + 2 * p) * 144 + 36 * g) * 1024;
  auto ks4 = [&](int j) { return j < 8 ? 8 * ((s + (j >> 1)) & 3) + 2 * (j & 1) : j < 16 ? 8 * ((s + j) & 3) + (j >> 2) * 2 : 2 * j; };
  unsigned ta[8], tz = L + 73728 + p * 2048 + vz, t4e, fv[4];
  u32x4 fc;
  ta[0] = L + row * 256 + (((4 * tf) ^ (row & 7)) * 32) + (c & 3) * 8;
  sfor<4>([&](auto I) { ta[I] = ta[0] ^ (I * 32); pin(ta[I]); }); pin(tz);
  bf16x8 Z[18][2], A[4], B[16][2];
  u32x2 Bs[2][2][2];
  f32x4 acc[9][2], ac4[9][2];
  auto rd_ = [&](auto I) {
    constexpr int i = I, wi = i >> 2, ph = phys(endidx(wi) - 1), hi = ph >= 9, so = (ph % 9) * 8192;
    if constexpr (i == 72) sfor<4>([&](auto K) { ta[K] = ((L + row * 128 + (((2 * tf) ^ ((row >> 1) & 3)) * 32) + (c & 3) * 8) ^ ((K & 1) * 32)) + (K >> 1) * 73728; pin(ta[K]); });
    if constexpr (i == 144) sfor<2>([&](auto K) { ta[K] = L + row * 64 + ((tf ^ ((row >> 2) & 1)) * 32) + (c & 3) * 8 + K * 73728; pin(ta[K]); });
    if constexpr (i == 162) { sfor<8>([&](auto K) { ta[K] = (L + tf * 73728 + row * 256 + (((row & 7) ^ (2 * g)) * 32) + (c & 3) * 8) ^ (K * 32); pin(ta[K]); }); t4e = L + L_SP + tf * 1024 + row * 32 + (c & 3) * 8; pin(t4e); }
    if constexpr (i < 72) {
      if constexpr ((i & 3) == 0) sfor<2>([&](auto K) { Z[wi][K] = wi < 16 ? ldsfrag(tz + so + K * 1024) : splitfrag(tz - lane * 8 + so + K * 1024); });
      A[i & 3] = trfrag(ta[i & 3] + so, 4096);
    } else if constexpr (i < 144) A[i & 3] = trfrag(ta[2 * hi + (i & 1)] + so + ((i >> 1) & 1) * 4096, 2048);
    else if constexpr (i < 162) A[i & 3] = trfrag(ta[hi] + so + (i & 3) * 2048, 1024);
    else { constexpr int j = (i - 162) / 9, u = (i - 162) % 9; if constexpr (u < 8) A[i & 3] = trfrag(ta[u] + ((3 + j) & 7) * 8192, 4096); else A[i & 3] = trfrag(t4e + (j & 7) * 2048, 512); }
  };
  TS(11);
  sfor<4>(rd_);
  float ca[2], cb[2], sg[2], gp;
  unsigned pk[4];
  auto gl = [&](auto V) {
    constexpr int v = V, pr = v >> 4, t2 = v < 64 ? (v >> 3) & 1 : (v >> 2) & 1, tl = v < 64 ? 2 * pr + ((v >> 2) & 1) : 8;
    const float o = gelu(acc[tl][t2][v & 3], ca[t2], cb[t2], sg[t2]);
    if constexpr (v & 1) pk[(v >> 1) & 3] = pk2(gp, o); else gp = o;
    if constexpr (v < 64 && (v & 7) == 7) st16c(hb + (t2 * 144 + 8 * s + (pr < 2 ? 2 * tf + pr : 2 * pr + tf)) * 1024, vz, u32x4{pk[0], pk[1], pk[2], pk[3]});
    if constexpr (v >= 64 && (v & 3) == 3) st8c(hb + (t2 * 144 + 32 + s) * 1024 + tf * 512, lane * 8, u32x2{pk[2 * t2], pk[2 * t2 + 1]});
  };
  const int fr = 8 * T + 2 * p + tf, t = 16 * fr + c, n0 = 128 * s + 32 * g + 4 * q, ne = 512 + 16 * s + 4 * q;
  unsigned yv[12], mu; u32x2 xf, st, ce, pe[3]; u32x4 xm, cm, pm[3];
  unsigned xo, xo2;
  auto finld = [&](auto I) {
    if constexpr (I < 12) ld4c<0>(yv[I], ws + O_YT + ((I & 3) + (I >> 2 == 1 ? 16 : 0)) * 4096, I < 8 ? n0 * 4096 + (fr >> 5) * 1024 + (fr & 15) * 64 + (fr & 16) / 2 + (c >> 2) * 16 + (c & 2) * 2 : ne * 4096 + (t >> 1) * 4);
    else if constexpr (I == 12) { unsigned l = vz; pin(l); xo = (16 * fr + (l >> 6)) * 1152; xo2 = xo + (l & 48) / 2; xo += l & 48; ld16<0>(xm, x + 256 * s + 64 * g, xo); }
    else if constexpr (I == 13) ld8<1024>(xf, x + 32 * s, xo2);
    else if constexpr (I == 15) ld8c<0>(st, ws + O_XST, t * 8);
    else if constexpr (I == 16) ld4c<0>(mu, ws + O_MU, t * 4);
    else if constexpr (I == 14) ld16c(cm, ws + O_XC + (size_t)(fr * 18 + 4 * s + g) * 1024, vz);
    else ld8c<0>(ce, ws + O_XC + (size_t)(fr * 36 + 32 + s) * 512, lane * 8);
  };
  auto step = [&](auto I) {
    constexpr int i = I, j = (i - 162) / 9, u = (i - 162) % 9;
    if constexpr (i < 162 ? (i + 4) % 8 == 0 : (u == 5 && j < 17)) {
      waitv<vcnt(i)>(); bar();
      if constexpr (i >= 156) { constexpr int jn = i < 162 ? 0 : j + 1; if constexpr (jn < 16) { pin(B[jn][0]); pin(B[jn][1]); } else sfor<4>([&](auto K) { pin(Bs[jn - 16][K >> 1][K & 1]); }); }
      if constexpr (i == PUB[0]) { if (tid == 64) st4c(fl + FC, b * 4, tag); }
      sfor<4>([&](auto E) {
        if constexpr (PUB[E] == i) { if (tid == 0) st4c(fl + F3 + E * 2048, fid, tag); }
        if constexpr (EXM[E] == i) { pin(fv[E]); if (!allq(fv[E], tag)) spin<unsigned>(fl + F3 + E * 2048, f3o, tag); }
      });
      if constexpr (EXM[4] == i) { pin(fc); if (!allq(fc, tag)) spin<u32x4>(fl + FC, vz, tag); }
    }
    const f32x4 z4 = {};
    if constexpr (i < 162) {
      constexpr int ks = i < 72 ? i >> 2 : i < 108 ? (i - 72) >> 1 : i < 144 ? (i - 108) >> 1 : i - 144;
      constexpr int tl = i < 72 ? i & 3 : i < 108 ? 4 + (i & 1) : i < 144 ? 6 + (i & 1) : 8;
      sfor<2>([&](auto K) { acc[tl][K] = mfma(A[i & 3], Z[ks][K], ks == 0 ? z4 : acc[tl][K]); });
    } else sfor<2>([&](auto K) {
      bf16x8 bf;
      if constexpr (j < 16) bf = B[j][K]; else bf = __builtin_bit_cast(bf16x8, __builtin_shufflevector(Bs[j - 16][K][0], Bs[j - 16][K][1], 0, 1, 2, 3));
      ac4[u][K] = mfma(A[i & 3], bf, j == 0 ? z4 : ac4[u][K]);
    });
    if constexpr (i + 4 < 324) rd_(IC<i + 4>{});
    if constexpr (i >= 4 && i < 162) { constexpr int G = (i + 4) / 8, k = (i + 4) % 8; if constexpr ((k & 1) && lim3(G - 1) + (k >> 1) < lim3(G)) issue(IC<lim3(G - 1) + (k >> 1)>{}); }
    sfor<36>([&](auto N) {
      constexpr int jj = N >> 1, k = N & 1;
      if constexpr (wIt(jj, k) == i) dma(L + (((3 + jj) & 7) + 9 * k) * 8192 + w * 1024, wk + 256 * s + (ks4(jj) + k) * 36864, vo);
      if constexpr (bIt(jj, k) == i) {
        const char* hp = hb + ((k ^ tf) * 144 + ks4(jj) + tf) * 1024;
        if constexpr (jj < 16) ld16c(B[jj][k], hp, vz); else { ld8c<0>(Bs[jj - 16][k][0], hp, lane * 8); ld8c<512>(Bs[jj - 16][k][1], hp, lane * 8); }
      }
    });
    sfor<5>([&](auto E) {
      if constexpr (eIt(E) == i) dma(L + L_SP + (E & 1) * 8192 + w * 1024, wk + 1024 + 32 * s + (ks4(E < 4 ? 4 * E + (w >> 1) : 16 + (p >> 1)) + (w & 1)) * 36864, ve);
      if constexpr (LDF[E] == i) { if constexpr (E < 4) ld4c<0>(fv[E], fl + F3 + E * 2048, f3o); else ld16c(fc, fl + FC, vz); }
    });
    if constexpr (i >= FIN && i < FIN + 18) finld(IC<i - FIN>{});
    if constexpr (i == PF4) { if (w >= 2) pf(L, wk + xi * 41472, min((int)vz * 8 + (w - 2) * 8192, 323 * 128)); }
    if constexpr (i >= CLR && i <= CLR + 12 && (i - CLR) % 4 == 0) {
      const u32x4 n4 = {~0u, ~0u, ~0u, ~0u};
      if constexpr (i < CLR + 12) st16c(ws + O_P + (size_t)((b * 3 + (i - CLR) / 4) * 8 + w) * 1024, vz, n4);
      else if (w < 3) st16c(ws + O_PE + (size_t)(b * 3 + w) * 1024, vz, n4);
    }
    if constexpr (i == 36) TX(12);
    if constexpr (i == 72) TX(13);
    if constexpr (i == 108) TX(14);
    if constexpr (i == 144) TX(15);
    if constexpr (i == 162) TS(16);
    if constexpr (i == 216) TX(17);
    if constexpr (i == 270) TX(18);
    if constexpr (i == 60) sfor<2>([&](auto J) {
      float y = 0;
      sfor<4>([&](auto K) { pin(sq[4 * J + K]); y += __builtin_bit_cast(float, sq[4 * J + K]); });
      const float vr = sumq(y) * (1.f / D_) + EPS, r = __builtin_amdgcn_rsqf(vr); sg[J] = vr * r; ca[J] = -GK * r; cb[J] = -GK * GC * r * r * r;
    });
    if constexpr (gV(i) >= 0) gl(IC<gV(i)>{});
    SB();
  };
  sfor<162>(step);
  sfor<162>([&](auto I) { step(IC<162 + I>{}); });
  TS(19);
  bar();
  sfor<9>([&](auto U) { *(LDS3 f32x4*)(L + w * 9216 + U * 1024 + vz) = ac4[U][1]; });
  barl();
  f32x4 z[9];
  sfor<9>([&](auto U) { z[U] = ac4[U][0] + *(LDS3 f32x4*)(L + (w ^ 4) * 9216 + U * 1024 + vz); });
  auto pz = [&](float a, float d) { const unsigned u = pk2(a, d); return u < 0xfffffffeu ? u : 0xfffffffeu; };
  sfor<3>([&](auto R) { constexpr int r = R + 1; st16c(ws + O_P + (size_t)(((b ^ r) * 3 + R) * 8 + w) * 1024, vz, u32x4{pz(z[2 * r][0], z[2 * r][1]), pz(z[2 * r][2], z[2 * r][3]), pz(z[2 * r + 1][0], z[2 * r + 1][1]), pz(z[2 * r + 1][2], z[2 * r + 1][3])}); });
  const bool own = p == g;
  if (!own) st8c(ws + O_PE + (size_t)(((b ^ p ^ g) * 3 + (p ^ g) - 1) * 2 + tf) * 512, lane * 8, u32x2{pz(z[8][0], z[8][1]), pz(z[8][2], z[8][3])});
  TS(20);
  waitv<3>(); pin(xm); pin(xf);
  const unsigned lt = L + 81920 + w * 2048, lq = lt + c * 64 + q * 8;
  *(LDS3 u32x4*)(lt + vz) = xm; *(LDS3 u32x2*)(lt + 1024 + lane * 8) = xf;
  u32x2 xa[3] = {*(LDS3 u32x2*)lq, *(LDS3 u32x2*)(lq + 32), *(LDS3 u32x2*)(lt + 1024 + c * 32 + q * 8)};
  sfor<3>([&](auto I) { pin(xa[I]); });
#pragma unroll 1
  for (int it = 0; it < (1 << 17); ++it) {
    asm volatile("global_load_dwordx4 %0, %6, %8 sc1\n\tglobal_load_dwordx4 %1, %6, %9 sc1\n\tglobal_load_dwordx4 %2, %6, %10 sc1\n\t"
                 "global_load_dwordx2 %3, %7, %11 sc1\n\tglobal_load_dwordx2 %4, %7, %11 offset:1024 sc1\n\tglobal_load_dwordx2 %5, %7, %11 offset:2048 sc1\n\ts_waitcnt vmcnt(0)"
                 : "=&v"(pm[0]), "=&v"(pm[1]), "=&v"(pm[2]), "=&v"(pe[0]), "=&v"(pe[1]), "=&v"(pe[2])
                 : "v"(vz), "v"(lane * 8), "s"(ws + O_P + (size_t)(b * 24 + w) * 1024), "s"(ws + O_P + (size_t)(b * 24 + 8 + w) * 1024), "s"(ws + O_P + (size_t)(b * 24 + 16 + w) * 1024), "s"(ws + O_PE + (size_t)(b * 6 + tf) * 512) : "memory");
    unsigned mx = 0;
    sfor<12>([&](auto I) { const unsigned u = pm[I >> 2][I & 3]; mx = u > mx ? u : mx; });
    sfor<6>([&](auto I) { const unsigned u = pe[I >> 1][I & 1]; mx = u > mx ? u : mx; });
    if (__builtin_amdgcn_ballot_w64(mx == ~0u) == 0) break;
  }
  TS(22);
  sfor<12>([&](auto I) { pin(yv[I]); }); pin(st); pin(mu); pin(cm); pin(ce);
  const f32x2 sf = __builtin_bit_cast(f32x2, st);
  auto fin = [&](auto H) {
    float r[4];
    sfor<4>([&](auto E) {
      constexpr int e = E, k = 2 * H + (e >> 1);
      auto X = [&](unsigned u) { return (e & 1) ? bhi(u) : blo(u); };
      float ob = __builtin_bit_cast(float, mu) + z[H < 2 ? H : 8][e];
      if constexpr (H < 2) ob += (X(cm[k]) + X(pm[0][k])) + (X(pm[1][k]) + X(pm[2][k])); else ob += (X(ce[k - 4]) + X(pe[0][k - 4])) + (X(pe[1][k - 4]) + X(pe[2][k - 4]));
      const float xf = X(xa[H][e >> 1]), y = (c & 1) ? bhi(yv[4 * H + e]) : blo(yv[4 * H + e]);
      r[e] = xf + (xf - sf[0]) * sf[1] * y * (ob * __builtin_amdgcn_rcpf(1.f + __builtin_amdgcn_exp2f(-1.44269504f * ob)));
    });
    *(LDS3 u32x2*)(H < 2 ? lq + 32 * H : lt + 1024 + c * 32 + q * 8) = u32x2{pk2(r[0], r[1]), pk2(r[2], r[3])};
  };
  fin(IC<0>{}); fin(IC<1>{});
  *(u32x4*)(out + 256 * s + 64 * g + xo) = *(LDS3 u32x4*)(lt + vz);
  if (own) { fin(IC<2>{}); *(u32x2*)(out + 1024 + 32 * s + xo2) = *(LDS3 u32x2*)(lt + 1024 + lane * 8); }
  TS(23);
  TSEND;
}

extern "C" void kernel_launch(void* const* d_in, const int* in_sizes, int n_in, void* d_out, int out_size, void* d_ws, size_t ws_size, hipStream_t stream) {
  k_all<<<256, 512, 0, stream>>>((const char*)d_in[0], (const char*)d_in[1], (const char*)d_in[2], (const char*)d_in[3], (const char*)d_in[4], (const unsigned*)d_in[5],
                                 (const char*)d_in[7], (const char*)d_in[8], (const char*)d_in[9], (const char*)d_in[10], (char*)d_ws, (char*)d_out);
}
```

```cpp
#include <hip/hip_runtime.h>
#include <hip/hip_bf16.h>
#include <utility>

typedef __attribute__((ext_vector_type(2))) __bf16 bf16x2;
typedef __attribute__((ext_vector_type(4))) __bf16 bf16x4;
typedef __attribute__((ext_vector_type(8))) __bf16 bf16x8;
typedef __attribute__((ext_vector_type(2))) float f32x2;
typedef __attribute__((ext_vector_type(4))) float f32x4;
typedef __attribute__((ext_vector_type(2))) unsigned u32x2;
typedef __attribute__((ext_vector_type(4))) unsigned u32x4;
#define LDS3 __attribute__((address_space(3)))
#define DEV __device__ __forceinline__
#define SB() __builtin_amdgcn_sched_barrier(0)

constexpr int S_ = 2048, D_ = 576, F_ = 4608;
constexpr float EPS = 1e-5f;
constexpr float GK = 2.302208198f;
constexpr float GC = 0.044715f;
constexpr size_t SZ_ACT = (size_t)S_ * D_ * 2;
constexpr size_t O_HS = 0, O_XC = O_HS + SZ_ACT, O_KVT = O_XC + SZ_ACT, O_YT = O_KVT + SZ_ACT,
                 O_P = O_YT + SZ_ACT, O_H = O_P + 4 * SZ_ACT, O_XST = O_H + 8 * SZ_ACT,
                 O_MU = O_XST + 16384, O_SS = O_MU + 8192, O_RM = O_SS + 131072, O_E = O_RM + 2048, O_F = O_E + 1024, O_PE = O_P + 3 * SZ_ACT;
constexpr int FK = 0, FC = 1024, F2 = 2048, F3 = 4096, F1 = F3 + 4 * 2048;

template <class F, int... I> DEV void sfor_(F&& f, std::integer_sequence<int, I...>) { (f(std::integral_constant<int, I>{}), ...); }
template <int N, class F> DEV void sfor(F&& f) { sfor_(f, std::make_integer_sequence<int, N>{}); }

DEV void dma(unsigned lds, const char* g, unsigned voff) {
  asm volatile("s_mov_b32 m0, %0\n\ts_nop 0\n\tglobal_load_lds_dwordx4 %1, %2" ::"s"(lds), "v"(voff), "s"(g) : "memory");
}
template <int N> DEV void waitv() { asm volatile("s_waitcnt vmcnt(%0)" ::"n"(N) : "memory"); }
DEV void bar() { asm volatile("s_barrier" ::: "memory"); }
DEV void barl() { asm volatile("s_waitcnt lgkmcnt(0)\n\ts_barrier" ::: "memory"); }
template <int O> DEV void ld8(u32x2& d, const char* g, unsigned voff) { asm volatile("global_load_dwordx2 %0, %1, %2 offset:%3" : "=v"(d) : "v"(voff), "s"(g), "n"(O) : "memory"); }
template <int O> DEV void ld16(u32x4& d, const char* g, unsigned voff) { asm volatile("global_load_dwordx4 %0, %1, %2 offset:%3" : "=v"(d) : "v"(voff), "s"(g), "n"(O) : "memory"); }
template <int N, class T> DEV void syncb(T& a, T& b) { asm volatile("s_waitcnt vmcnt(%2)\n\ts_barrier" : "+v"(a), "+v"(b) : "n"(N) : "memory"); }
#define TS(i)
#define TSDECL
#define TSEND
DEV void dmac(unsigned lds, const char* g, unsigned voff) {
  asm volatile("s_mov_b32 m0, %0\n\ts_nop 0\n\tglobal_load_lds_dwordx4 %1, %2 sc1" ::"s"(lds), "v"(voff), "s"(g) : "memory");
}
template <int O> DEV void ld4(unsigned& d, const char* g, unsigned voff) { asm volatile("global_load_dword %0, %1, %2 offset:%3" : "=v"(d) : "v"(voff), "s"(g), "n"(O) : "memory"); }
template <int O> DEV void ld4c(unsigned& d, const char* g, unsigned voff) { asm volatile("global_load_dword %0, %1, %2 offset:%3 sc1" : "=v"(d) : "v"(voff), "s"(g), "n"(O) : "memory"); }
template <int O> DEV void ld8c(u32x2& d, const char* g, unsigned voff) { asm volatile("global_load_dwordx2 %0, %1, %2 offset:%3 sc1" : "=v"(d) : "v"(voff), "s"(g), "n"(O) : "memory"); }
template <class T> DEV void ld16c(T& d, const char* g, unsigned voff) { asm volatile("global_load_dwordx4 %0, %1, %2 sc1" : "=v"(d) : "v"(voff), "s"(g) : "memory"); }
DEV void st4c(const char* g, unsigned voff, unsigned d) { asm volatile("global_store_dword %0, %1, %2 sc1" ::"v"(voff), "v"(d), "s"(g) : "memory"); }
DEV void st8c(const char* g, unsigned voff, u32x2 d) { asm volatile("global_store_dwordx2 %0, %1, %2 sc1" ::"v"(voff), "v"(d), "s"(g) : "memory"); }
template <int O = 0> DEV void st16c(const char* g, unsigned voff, u32x4 d) { asm volatile("global_store_dwordx4 %0, %1, %2 offset:%3 sc1\n\ts_nop 1" ::"v"(voff), "v"(d), "s"(g), "n"(O) : "memory"); }
DEV bool allq(unsigned v, unsigned tag) { return __builtin_amdgcn_ballot_w64(v != tag) == 0; }
DEV bool allq(u32x4 v, unsigned tag) { return __builtin_amdgcn_ballot_w64(((v[0] ^ tag) | (v[1] ^ tag) | (v[2] ^ tag) | (v[3] ^ tag)) != 0) == 0; }
template <class T> DEV void spin(const char* g, unsigned voff, unsigned tag) {
#pragma unroll 1
  for (int it = 0; it < (1 << 17); ++it) {
    T v;
    if constexpr (sizeof(T) == 4) asm volatile("global_load_dword %0, %1, %2 sc1\n\ts_waitcnt vmcnt(0)" : "=&v"(v) : "v"(voff), "s"(g) : "memory");
    else asm volatile("global_load_dwordx4 %0, %1, %2 sc1\n\ts_waitcnt vmcnt(0)" : "=&v"(v) : "v"(voff), "s"(g) : "memory");
    if (allq(v, tag)) break;
    __builtin_amdgcn_s_sleep(2);
  }
}
template <class T> DEV void pin(T& d) { asm volatile("" : "+v"(d)); }

DEV bf16x8 trfrag(unsigned a, int second) {
  bf16x4 x = __builtin_amdgcn_ds_read_tr16_b64_v4bf16((LDS3 bf16x4*)a);
  bf16x4 y = __builtin_amdgcn_ds_read_tr16_b64_v4bf16((LDS3 bf16x4*)(a + second));
  return __builtin_shufflevector(x, y, 0, 1, 2, 3, 4, 5, 6, 7);
}
DEV bf16x8 ldsfrag(unsigned a) { return *(LDS3 bf16x8*)a; }
DEV f32x4 mfma(bf16x8 a, bf16x8 b, f32x4 c) { return __builtin_amdgcn_mfma_f32_16x16x32_bf16(a, b, c, 0, 0, 0); }
DEV unsigned pk2(float a, float b) { f32x2 v = {a, b}; return __builtin_bit_cast(unsigned, __builtin_convertvector(v, bf16x2)); }
DEV float blo(unsigned u) { return __builtin_bit_cast(float, u << 16); }
DEV float bhi(unsigned u) { return __builtin_bit_cast(float, u & 0xffff0000u); }
DEV float gelu(float u, float ca, float cb, float sg) {
  float t = u * u; float p = __builtin_fmaf(t, cb, ca); float e = __builtin_amdgcn_exp2f(u * p);
  float o = u * __builtin_amdgcn_rcpf(__builtin_fmaf(e, sg, sg)); pin(o); return o;
}
template <int C> DEV float dpp(float v) { return __builtin_bit_cast(float, __builtin_amdgcn_update_dpp(0, __builtin_bit_cast(int, v), C, 0xf, 0xf, true)); }
DEV float sum16(float v) { v += dpp<0x128>(v); v += dpp<0x124>(v); v += dpp<0x122>(v); v += dpp<0x121>(v); return v; }
DEV float sum8(float v) { v += dpp<0xB1>(v); v += dpp<0x4E>(v); v += dpp<0x141>(v); return v; }
DEV float dsum(unsigned u, float a) { return __builtin_amdgcn_fdot2_f32_bf16(__builtin_bit_cast(bf16x2, u), __builtin_bit_cast(bf16x2, 0x3f803f80u), a, false); }
DEV float sumq(float v) { v += __shfl_xor(v, 16); v += __shfl_xor(v, 32); return v; }

DEV bf16x8 splitfrag(unsigned a) { const u32x2 x = *(LDS3 u32x2*)a, y = *(LDS3 u32x2*)(a + 512); return __builtin_bit_cast(bf16x8, u32x4{x[0], x[1], y[0], y[1]}); }
template <int I> using IC = std::integral_constant<int, I>;
#define TX(i)

constexpr int L_CV = 131072, CV_SZ = 8736, CV_U = 512, CV_H = 4640, L_EXT = 100352, L_ACT = 118784, L_HS = 73728, L_EX2 = 110592, L_RM = 129024, L_RB = 98304, L_SP = 147456, L_SC = 98304, L_JK = 163584;
DEV void pf(unsigned L, const char* g, unsigned voff) { asm volatile("s_mov_b32 m0, %0\n\ts_nop 0\n\tglobal_load_lds_dword %1, %2" ::"s"(L + L_JK), "v"(voff), "s"(g) : "memory"); }
DEV bf16x8 toep(unsigned a, unsigned sh) {
  const LDS3 unsigned* hp = (const LDS3 unsigned*)a;
  const unsigned d0 = hp[0], d1 = hp[1], d2 = hp[2], d3 = hp[3], d4 = hp[4];
  return __builtin_bit_cast(bf16x8, u32x4{__builtin_amdgcn_alignbyte(d1, d0, sh), __builtin_amdgcn_alignbyte(d2, d1, sh), __builtin_amdgcn_alignbyte(d3, d2, sh), __builtin_amdgcn_alignbyte(d4, d3, sh)});
}
constexpr int imin(int a, int b) { return a < b ? a : b; }
constexpr int imax(int a, int b) { return a > b ? a : b; }
constexpr int endidx(int wi) { return wi < 0 ? 0 : wi < 18 ? 2 * wi + 2 : wi > 40 ? 59 : 19 + wi; }
constexpr int lim3(int G) { return imin(59, 18 + endidx(2 * G - 2)); }
constexpr int phys(int n) { return ((n % 18) & 1) ? (n % 18) >> 1 : 9 + ((n % 18) >> 1); }
constexpr int slotIt(int n) { int G = 1; while (lim3(G) <= n) ++G; return 8 * G - 3 + 2 * (n - lim3(G - 1)); }
constexpr int PUB[4] = {124, 176, 212, 230}, LDF[5] = {143, 190, 226, 250, 216}, EXM[5] = {156, 203, 239, 266, 239};
constexpr int CODE = 54784, FIN = 284, CLR = 42, PF4 = -1;
constexpr int gI(int v) { return v < 32 ? 74 + v * 4 / 3 : v < 48 ? 118 + (v - 32) * 11 / 4 : 162 + (v - 48) * 5 / 2; }
constexpr int gV(int n) { for (int v = 0; v < 72; ++v) if (gI(v) == n) return v; return -1; }
constexpr int wIt(int j, int k) { return j < 5 ? 109 + 8 * j + 2 * k : 162 + 9 * (j - 5) + 2 + k; }
constexpr int bIt(int j, int k) { return j < 2 ? PUB[0] + 2 + 2 * j + k : j < 4 ? EXM[0] - 3 + 2 * j + k : 162 + 9 * (j - 4) + k; }
constexpr int eIt(int e) { return e < 2 ? 101 + 30 * e : 162 + 9 * (4 * e - 4) + 6; }
constexpr int nops(int n) {
  if (n < 0) return n == -2 ? 8 : 1;
  int c = 0;
  for (int i = 18; i < 59; ++i) c += slotIt(i) == n;
  for (int j = 0; j < 18; ++j) for (int k = 0; k < 2; ++k) c += (wIt(j, k) == n) + (bIt(j, k) == n) * (j < 16 ? 1 : 2);
  for (int e = 0; e < 5; ++e) c += (eIt(e) == n) + (LDF[e] == n);
  c += (n >= FIN && n < FIN + 18) + (n == CLR || n == CLR + 4 || n == CLR + 8);
  const int v = gV(n);
  return c + (v >= 0 && (v < 64 ? (v & 7) == 7 : (v & 3) == 3));
}
constexpr int need4(int j) { return imax(imax(wIt(j, 1), bIt(j, 1)), eIt(j / 4)); }
constexpr int vcnt(int n) {
  int l = 0;
  if (n < 162) { const int e = endidx(2 * ((n + 4) / 8) + 1); l = e <= 18 ? -12 + e / 2 : slotIt(e - 1); if (n == 156) l = imax(l, need4(0)); }
  else l = need4((n - 162) / 9 + 1);
  for (int e = 0; e < 5; ++e) { if (e < 4 && PUB[e] == n) l = imax(l, gI(e < 3 ? 31 + 16 * e : 71)); if (EXM[e] == n) l = imax(l, LDF[e]); }
  int c = 0;
  for (int m = l + 1; m < n; ++m) c += nops(m);
  return c;
}

__global__ __launch_bounds__(512, 2) void k_all(const char* __restrict__ x, const char* __restrict__ kk, const char* __restrict__ vv, const char* __restrict__ shk,
    const char* __restrict__ ssk, const unsigned* __restrict__ ssd, const char* __restrict__ w1, const char* __restrict__ w2, const char* __restrict__ w3,
    const char* __restrict__ w4, char* __restrict__ ws, char* __restrict__ out) {
  __shared__ __attribute__((aligned(1024))) char smem[163840];
  const unsigned L = (unsigned)(size_t)(LDS3 char*)smem;
  const int b = blockIdx.x, tid = threadIdx.x, lane = tid & 63, w = __builtin_amdgcn_readfirstlane(tid >> 6);
  const int g = b & 3, T = ((b >> 2) & 1) * 8 + (b >> 5), s = (b >> 3) & 3, T32 = 4 * T + g, g2 = s, xi = b >> 3;
  const int q = lane >> 4, c = lane & 15, tf = w >> 2, p = w & 3, row = 4 * q + (c >> 2);
  const char* fl = ws + O_F;
  const int fid = (T * 32 + g * 4 + s) * 4, ch3 = 512 + (b >> 2), chA = 8 * (b >> 2) + g;
  TSDECL;
  if (w == 7) { const char* pc; asm volatile("s_getpc_b64 %0" : "=s"(pc)); pf(L, pc, min(((xi & 7) * 64 + lane) * 128, CODE)); }
  if (w == 5) sfor<3>([&](auto I) { pf(L, w1 + 20736 * xi, min(lane + 64 * I, 161) * 128); });
  unsigned tag;
  const unsigned vz = lane * 16;
  const bool ext = p == tf;
  const int rr = 4 * w + q;
  const unsigned vo = rr * 1152 + ((c ^ ((rr & 7) << 1)) * 16), ve = (lane >> 1) * 1152 + (lane & 1) * 16;
  const unsigned t0 = L + row * 256 + (((2 * p) ^ (row & 7)) * 32) + (c & 3) * 8, t1 = t0 ^ 32, te = L + L_EXT + row * 32 + (c & 3) * 8;
  const char* wm2 = w2 + 256 * g2;
  {
    const char* wm = w1 + 288 * g2 + 32 * (g2 & 1);
    const char* we = w1 + 288 * g2 + ((g2 & 1) ? 0 : 256);
    u32x2 ar[19]; u32x4 sk[2];
    const int kb = 36 * g2 + 12 * p, kd = kb + min(c, 11), kt = 32 * T32 + 8 * q;
    const int rmr = (q < 2 || b >= 32) ? 2 * b + (q & 1) : 510 + 2 * b + q;
    const int r = tid >> 3, e = tid & 7, srow = 32 * T32 + r + ((g2 & 1) ? 1 : -1) * (1 + (g2 >> 1));
    const bool ok = srow >= 0 && srow < S_;
    f32x2 xs = {};
    unsigned xc[36];
    if (w < 4) {
      u32x4 xv[9];
      { const unsigned xo = (unsigned)(ok ? srow : 0) * 1152 + e * 16; sfor<9>([&](auto I) { ld16<I * 128>(xv[I], x, xo); }); }
      ld4c<0>(tag, ws + O_E, 0u);
      const unsigned za = L + L_ACT + r * 1184 + (e >> 2) * 64 + (e & 1) * 32 + (e & 2) * 4;
      bar(); waitv<0>();
      TS(1);
      sfor<9>([&](auto I) { pin(xv[I]); }); pin(tag);
      float sm[4] = {};
      sfor<36>([&](auto I) { sm[I & 3] = dsum(xv[I >> 2][I & 3], sm[I & 3]); });
      const float m = sum8((sm[0] + sm[1]) + (sm[2] + sm[3])) * (1.f / D_);
      sfor<36>([&](auto I) { const unsigned u = xv[I >> 2][I & 3]; xc[I] = pk2(blo(u) - m, bhi(u) - m); if constexpr (I & 1) *(LDS3 u32x2*)(za + (I >> 2) * 128 + (I & 2) * 8) = u32x2{xc[I - 1], xc[I]}; });
      xs[0] = m;
    } else {
      ld4c<0>(tag, ws + O_E, 0u);
      bar();
      sfor<12>([&](auto N) {
        dma(L + N * 8192 + p * 1024, wm + (size_t)N * 32 * 1152, vo - 18432); dma(L + N * 8192 + w * 1024, wm + (size_t)N * 32 * 1152, vo);
        if (p == (N & 3)) dma(L + L_EXT + N * 1024, we + (size_t)N * 32 * 1152, ve);
      });
      TS(13);
      waitv<24>(); pin(tag);
    }
    const bool kw = w < 7;
    const char* b0 = kw ? kk : w2;
    const int o0 = kw ? (kt - 3) * 1152 + kd * 8 : rmr * 1152 + c * 8, s0 = kw ? 1152 : 128, m0 = kw ? 1 << 30 : o0 + 1024, o1 = kw ? o0 + 3456 : 0;
    tag += 1;
    f32x4 a0 = {}, a1 = {}, ae = {};
    const unsigned bb = L + L_ACT + (16 * tf + c) * 1184 + q * 16;
    TS(2);
    bf16x8 A0[6], A1[6];
    auto rdw = [&](auto K) { sfor<6>([&](auto I) { constexpr int so = ((K + I) % 12) * 8192; A0[I] = trfrag(t0 + so, 4096); A1[I] = trfrag(t1 + so, 4096); }); };
    auto mm = [&](auto K) {
      bf16x8 B[6];
      sfor<6>([&](auto I) { B[I] = ldsfrag(bb + (K + I) * 64); });
      if (ext) { bf16x8 E[6]; sfor<6>([&](auto I) { E[I] = trfrag(te + (K + I) * 1024, 512); }); sfor<6>([&](auto I) { ae = mfma(E[I], B[I], ae); }); }
      sfor<6>([&](auto I) { a0 = mfma(A0[I], B[I], a0); a1 = mfma(A1[I], B[I], a1); });
    };
    waitv<13>(); barl();
    TS(15);
    rdw(IC<0>{});
    waitv<0>(); barl();
    sfor<6>([&](auto I) { dma(L + I * 8192 + w * 1024, wm + (size_t)(12 + I) * 32 * 1152, vo); if (w == I) dma(L + L_EXT + (12 + I) * 1024, we + (size_t)(12 + I) * 32 * 1152, ve); });
    if (!tf) pf(L, w2 + 20736 * xi, min(tid, 161) * 128);
    mm(IC<0>{}); rdw(IC<6>{}); mm(IC<6>{});
    TS(17);
    if (tf) {
      sfor<11>([&](auto I) { ld8<0>(ar[I], b0, min(max(o0 + I * s0, 0), m0)); });
      sfor<8>([&](auto I) { ld8<(I % 3) * 1152>(ar[11 + I], vv, o1 + (I / 3) * 3456); });
      ld16<0>(sk[0], shk, min(kd, 143) * 32); ld16<16>(sk[1], shk, min(kd, 143) * 32);
      waitv<21>();
    } else {
      float v4[4] = {};
      sfor<36>([&](auto I) { const bf16x2 y = __builtin_bit_cast(bf16x2, xc[I]); v4[I & 3] = __builtin_amdgcn_fdot2_f32_bf16(y, y, v4[I & 3], false); });
      const float v = sum8((v4[0] + v4[1]) + (v4[2] + v4[3])) * (1.f / D_), rs = __builtin_amdgcn_rsqf(v + EPS);
      *(LDS3 float*)(L + L_SC + r * 4) = ok ? rs * __builtin_amdgcn_rsqf(v * rs * rs + EPS) : 0.f;
      xs[1] = rs;
      waitv<1>();
    }
    barl();
    TS(18);
    rdw(IC<12>{});
    const float sc = *(LDS3 float*)(L + L_SC + (16 * tf + c) * 4);
    sfor<6>([&](auto I) { dma(L + (6 + I) * 8192 + w * 1024, wm2 + (size_t)I * 32 * 1152, vo); });
    mm(IC<12>{});
    TS(3);
    if (tf) {
      waitv<6>();
      TS(21);
      sfor<19>([&](auto I) { pin(ar[I]); }); pin(sk[0]); pin(sk[1]);
      if (kw) {
        const int l2 = min(lane, 47), sl = ((l2 & 3) * 16 + (l2 >> 2)) * 4;
        if (kt == 0) ar[0] = ar[1] = ar[2] = u32x2{0, 0};
        sfor<2>([&](auto H) {
          auto X = [&](const u32x2& u) { const unsigned y = u[(int)H]; return f32x2{blo(y), bhi(y)}; };
          const u32x4 sh = sk[H];
          const f32x2 c3 = {blo(sh[0]), blo(sh[2])}, c2 = {bhi(sh[0]), bhi(sh[2])}, c1 = {blo(sh[1]), blo(sh[3])}, c0 = {bhi(sh[1]), bhi(sh[3])};
          f32x2 kf[11], r0[8];
          sfor<11>([&](auto I) { kf[I] = X(ar[I]); });
          sfor<8>([&](auto I) { r0[I] = (kf[I + 3] * c3 + kf[I + 2] * c2 + kf[I + 1] * c1 + kf[I] * c0) * X(ar[11 + I]); });
          sfor<2>([&](auto E) {
            auto P = [&](f32x2 y0, f32x2 y1) { return (unsigned)__builtin_amdgcn_ds_bpermute(sl, (int)pk2(y0[(int)E], y1[(int)E])); };
            st16c<0>(ws + O_KVT + (2 * H + E) * 9216 + T32 * 36864 + kb * 64, l2 * 16, u32x4{P(r0[7], r0[6]), P(r0[5], r0[4]), P(r0[3], r0[2]), P(r0[1], r0[0])});
          });
        });
      } else {
        float z = 0;
        sfor<18>([&](auto I) { z = dsum(ar[I >> 1][I & 1], z); });
        z = sum16(z) * (1.f / D_);
        const float z1 = __shfl_xor(z, 16);
        if ((lane & 31) == 0) { const int k = rmr & 31; st4c(ws + O_RM, ((rmr >> 5) * 32 + ((k >> 2) & 3) * 8 + (k >> 4) * 4 + (k & 3)) * 2, pk2(z, z1)); }
      }
      TS(12);
    }
    const float ga = -GK * sc, gb = -GK * GC * sc * sc * sc, gs = __builtin_amdgcn_rcpf(sc);
    auto G = [&](float u) { return gelu(u, ga, gb, gs); };
    const char* hs = ws + O_HS + (size_t)(2 * T32 + tf) * 18 * 1024;
    st16c(hs + (((9 * g2 + 1) >> 1) + p) * 1024, vz, u32x4{pk2(G(a0[0]), G(a0[1])), pk2(G(a0[2]), G(a0[3])), pk2(G(a1[0]), G(a1[1])), pk2(G(a1[2]), G(a1[3]))});
    if (ext) st8c(hs + ((g2 & 1) ? 9 * g2 : 9 * g2 + 8) * 512, lane * 8, u32x2{pk2(G(ae[0]), G(ae[1])), pk2(G(ae[2]), G(ae[3]))});
    if (!tf && g2 < 2 && ok && e == 0) st8c(ws + O_XST, srow * 8, __builtin_bit_cast(u32x2, xs));
    bar();
    sfor<3>([&](auto I) { dma(L + I * 8192 + w * 1024, wm2 + (size_t)(6 + I) * 32 * 1152, vo); });
    waitv<3>(); bar();
    if (tid == 0) st4c(fl + FK, b * 4, tag);
    TS(4);
  }
  const char* wa = w3 + 2304 * g + 512 * s;
  const char* wk = w4 + (size_t)1327104 * g;
  const char* zs = ws + O_XC + (size_t)(8 * T + w) * 18 * 1024;
  const unsigned f3o = (T * 32 + g * 4 + (lane & 3)) * 4;
  const unsigned va = rr * 9216 + ((c ^ ((rr & 7) << 1)) * 16);
  {
    const int t = 32 * T32 + 16 * tf + c;
    sfor<3>([&](auto I) { dma(L + (3 + I) * 8192 + w * 1024, wm2 + (size_t)(9 + I) * 32 * 1152, vo); });
    sfor<3>([&](auto I) { const int n = min(8 * I + w, 17); dma(L + L_EX2 + n * 1024, w2 + 1024 + 32 * g2 + (size_t)n * 32 * 1152, ve); });
    u32x2 st, xf; u32x4 xm;
    { const unsigned xo = (32 * T32 + 16 * tf + (lane >> 2)) * 1152; ld16<0>(xm, x + 256 * g2 + 64 * p, xo + (lane & 3) * 16); ld8<1024>(xf, x + 32 * g2, xo + (lane & 3) * 8); }
    bf16x8 A0[18], A1[18];
    auto rda = [&](auto I) { constexpr int so = (I < 9 ? (I + 6) % 12 : (I - 6) % 9) * 8192; A0[I] = trfrag(t0 + so, 4096); A1[I] = trfrag(t1 + so, 4096); };
    sfor<6>(rda);
    waitv<8>(); barl();
    sfor<3>([&](auto I) { dma(L + (6 + I) * 8192 + w * 1024, wm2 + (size_t)(12 + I) * 32 * 1152, vo); rda(IC<6 + I>{}); });
    if (w < 3 && lane < 34) *(LDS3 u32x4*)(L + L_CV + w * CV_SZ + (lane < 32 ? lane * 16 : 4096 + lane * 16)) = u32x4{0, 0, 0, 0};
    barl();
    sfor<3>([&](auto I) { dma(L + I * 8192 + w * 1024, wm2 + (size_t)(15 + I) * 32 * 1152, vo); });
    if (w == 0) spin<u32x4>(fl + FK, vz, tag);
    else waitv<0>();
    bar();
    TS(5);
    if (b == 0 && tid == 0) st4c(ws + O_E, 0u, tag);
    ld8c<0>(st, ws + O_XST, t * 8);
    sfor<5>([&](auto I) {
      const int n = min(8 * I + w, 37);
      dmac(L + (n < 36 ? L_HS : L_RM - 36864) + n * 1024, ws + (n < 36 ? O_HS + (size_t)36864 * T32 : O_RM - 36864) + n * 1024, vz);
    });
    bar();
    sfor<3>([&](auto I) {
      const int ch = I < 2 ? chA + 4 * I : ch3;
      dmac(L + L_CV + I * CV_SZ + (tf ? CV_H : CV_U) + p * 1024, tf ? ssk + (size_t)ch * 4096 + p * 1024 : ws + O_KVT + (size_t)16 * p * 36864 + (ch & 3) * 9216 + (ch >> 2) * 64, tf ? vz : (lane >> 2) * 36864 + (lane & 3) * 16);
    });
    sfor<9>([&](auto I) { rda(IC<9 + I>{}); });
    waitv<3>(); barl();
    if (w >= 2) { const int i = min(tid - 128, 323), i2 = i / 18; pf(L, w3 + 2304 * g, (18 * xi + i2) * 9216 + (i - 18 * i2) * 128); }
    TS(6);
    pin(xm); pin(xf);
    *(LDS3 u32x4*)(L + w * 2048 + vz) = xm; *(LDS3 u32x2*)(L + w * 2048 + 1024 + lane * 8) = xf;
    const u32x2 x0 = *(LDS3 u32x2*)(L + w * 2048 + c * 64 + q * 8), x1 = *(LDS3 u32x2*)(L + w * 2048 + c * 64 + q * 8 + 32), xe = *(LDS3 u32x2*)(L + w * 2048 + 1024 + c * 32 + q * 8);
    f32x4 a0 = {}, a1 = {}, ae = {}, am = {};
    const unsigned bb = L + L_HS + tf * 18432, tm = L + L_RM + q * 16;
    auto gm = [&](auto X) {
      bf16x8 B[18], M[18], E[18];
      auto rd = [&](auto I) { B[I] = (I == 4 || I == 13) ? splitfrag(bb + I * 1024 + lane * 8) : ldsfrag(bb + I * 1024 + vz); M[I] = ldsfrag(tm + I * 64); if constexpr (X) E[I] = trfrag(te + (L_EX2 - L_EXT) + I * 1024, 512); };
      sfor<3>(rd);
      sfor<18>([&](auto I) {
        if constexpr (I + 3 < 18) rd(IC<I + 3>{});
        a0 = mfma(A0[I], B[I], a0); a1 = mfma(A1[I], B[I], a1); am = mfma(M[I], B[I], am);
        if constexpr (X) ae = mfma(E[I], B[I], ae);
        SB();
      });
    };
    if (ext) gm(IC<1>{}); else gm(IC<0>{});
    TS(7);
    pin(st);
    const f32x2 sf = __builtin_bit_cast(f32x2, st); const float m = sf[0], rs = sf[1], mu = am[0];
    float ss = 0;
    auto E = [&](u32x2 xv, f32x4 a) {
      const float v0 = (blo(xv[0]) - m) * rs + a[0] - mu, v1 = (bhi(xv[0]) - m) * rs + a[1] - mu, v2 = (blo(xv[1]) - m) * rs + a[2] - mu, v3 = (bhi(xv[1]) - m) * rs + a[3] - mu;
      ss += (v0 * v0 + v1 * v1) + (v2 * v2 + v3 * v3);
      return u32x2{pk2(v0, v1), pk2(v2, v3)};
    };
    const char* xc = ws + O_XC + (size_t)(2 * T32 + tf) * 18 * 1024;
    const u32x2 o0 = E(x0, a0), o1 = E(x1, a1);
    st16c(xc + (4 * g2 + p) * 1024, vz, u32x4{o0[0], o0[1], o1[0], o1[1]});
    if (ext) st8c(xc + (32 + g2) * 512, lane * 8, E(xe, ae));
    ss = sumq(ss);
    if (lane < 16) { st4c(ws + O_SS + (2 * T32 + tf) * 1024 + (4 * g2 + p) * 64, lane * 4, __builtin_bit_cast(unsigned, ss)); if (g2 == 0 && p == 0) st4c(ws + O_MU, t * 4, __builtin_bit_cast(unsigned, mu)); }
    waitv<0>();
    if (w == 4 && lane < 3) {
      const unsigned d0 = ssd[chA >> 1], d1 = ssd[(chA >> 1) + 2], d3 = ssd[ch3 >> 1], dd = lane == 0 ? d0 : lane == 1 ? d1 : d3;
      LDS3 unsigned short* hp = (LDS3 unsigned short*)(L + L_CV + lane * CV_SZ + CV_H);
      *hp = (unsigned short)(pk2(blo(*hp) + (((lane < 2 ? chA : ch3) & 1) ? bhi(dd) : blo(dd)), 0.f) & 0xffff);
    }
    barl();
    if (tid == 0) st4c(fl + F2, fid, tag);
    TS(8);
  }
  f32x4 ys[2];
  {
    const int st = 9 + c - 8 * q;
    const unsigned sh = (st & 1) * 2, ho = L + L_CV + CV_H + (st >> 1) * 4, uo = L + L_CV + CV_U + (c - 1) * 32 + q * 16;
    f32x4 acc[8] = {}, c0 = {}, c1 = {};
    sfor<2>([&](auto S) {
      const unsigned ha = ho + tf * CV_SZ + (2 * p + S) * 64, ua = uo + tf * CV_SZ - (2 * p + S) * 64;
      bf16x8 A[8], B[8];
      A[0] = toep(ha, sh);
      sfor<8>([&](auto M) { B[M] = ldsfrag(ua + M * 512); });
      sfor<8>([&](auto M) {
        if constexpr (M < 4) A[M + 1] = toep(ha + (M + 1) * 512, sh); else if constexpr (M == 4) sfor<3>([&](auto I) { A[5 + I] = toep(ha + (5 + I) * 512, sh); });
        sfor<8 - M>([&](auto I) { acc[M + I] = mfma(A[M], B[I], acc[M + I]); });
        if constexpr (M < 6 - 3 * S) dma(L + (6 * S + M) * 8192 + w * 1024, wa + (size_t)(6 * S + M) * 32 * 9216, va);
        if constexpr (PF4 < 0 && S == 1 && M == 3) { if (w >= 2) pf(L, wk + xi * 41472, min(tid - 128, 323) * 128); }
      });
    });
    unsigned f2;
    ld4c<0>(f2, fl + F2 + T * 128, c * 4);
    { const unsigned ha = ho + 2 * CV_SZ + w * 64, ua = uo + 2 * CV_SZ - w * 64, za = L + L_CV + (lane & 31) * 16;
      sfor<8>([&](auto M) {
        const bf16x8 A = toep(ha + M * 512, sh);
        c1 = mfma(A, ldsfrag(M < 5 || 7 - g >= M ? ua + (7 - g - M) * 512 : za), c1);
        if constexpr (M < 4) c0 = mfma(A, ldsfrag(g >= M ? ua + (g - M) * 512 : za), c0);
      }); }
    TS(9);
    waitv<0>(); pin(f2);
    if (!allq(f2, tag)) spin<unsigned>(fl + F2 + T * 128, c * 4, tag);
    sfor<3>([&](auto I) { dmac(L + (9 + I) * 8192 + w * 1024, zs + I * 1024, vz); });
    barl();
    TS(10);
    const unsigned ra = L + L_RB + tf * 24576 + vz;
    sfor<4>([&](auto P) { if (p == P) {
      sfor<6>([&](auto I) { constexpr int r = 1 + (I >> 1), d = P ^ r; *(LDS3 f32x4*)(ra + (d * 3 + r - 1) * 2048 + (I & 1) * 1024) = acc[2 * d + (I & 1)]; });
      ys[0] = acc[2 * P]; ys[1] = acc[2 * P + 1];
    } });
    const unsigned rc = L + L_RB + 49152 + vz;
    if (w < 7) { *(LDS3 f32x4*)(rc + w * 2048) = c0; *(LDS3 f32x4*)(rc + w * 2048 + 1024) = c1; }
    barl();
    sfor<6>([&](auto I) { ys[I & 1] += *(LDS3 f32x4*)(ra + p * 6144 + I * 1024); });
    if (w == 7) sfor<7>([&](auto I) { c0 += *(LDS3 f32x4*)(rc + I * 2048); c1 += *(LDS3 f32x4*)(rc + I * 2048 + 1024); });
    pin(ys[0]); pin(ys[1]); pin(c0); pin(c1);
    waitv<1>();
    barl();
    if (w == 7) {
      st8c(ws + O_YT + (size_t)ch3 * 4096 + g * 512, c * 32 + q * 8, u32x2{pk2(c0[0], c0[1]), pk2(c0[2], c0[3])});
      st8c(ws + O_YT + (size_t)ch3 * 4096 + (7 - g) * 512, c * 32 + q * 8, u32x2{pk2(c1[0], c1[1]), pk2(c1[2], c1[3])});
    }
  }
  unsigned sq[8];
  sfor<6>([&](auto I) { dmac(L + (12 + I) * 8192 + w * 1024, zs + (3 + I) * 1024, vz); });
  TS(14);
  sfor<8>([&](auto I) { ld4c<(I >> 2) * 1024 + (I & 3) * 64>(sq[I], ws + O_SS + (8 * T + 2 * p) * 1024, q * 256 + c * 4); });
  st16c(ws + O_YT + (size_t)(chA + 4 * tf) * 4096 + p * 1024, c * 64 + q * 16, u32x4{pk2(ys[0][0], ys[0][1]), pk2(ys[0][2], ys[0][3]), pk2(ys[1][0], ys[1][1]), pk2(ys[1][2], ys[1][3])});
  const char* wd = w3 + 2304 * g + 2048 + 64 * s;
  const int rb = 8 * w + (lane >> 3), rd = 16 * w + (lane >> 2);
  const unsigned vb = rb * 9216 + (((lane & 7) ^ (((rb >> 1) & 3) << 1)) * 16), vd = rd * 9216 + (((lane & 3) ^ (((rd >> 2) & 1) << 1)) * 16);
  auto issue = [&](auto N) {
    constexpr int n = N; const unsigned dst = L + phys(n) * 8192 + w * 1024;
    if constexpr (n < 36) { if constexpr (n & 1) dma(dst, wa + (size_t)(n >> 1) * 32 * 9216, va); else dmac(dst, zs + (n >> 1) * 1024, vz); }
    else if constexpr (n < 45) dma(dst, wa + 256 + (size_t)(n - 36) * 64 * 9216, vb);
    else if constexpr (n < 54) dma(dst, wa + 384 + (size_t)(n - 45) * 64 * 9216, vb);
    else if constexpr (n < 58) dma(dst, wd + (size_t)(n - 54) * 128 * 9216, vd);
    else dma(dst, wd + (size_t)(tf ? 448 : 512) * 9216, vd);
  };
  const char* hb = ws + O_H + ((size_t)(8 * T + 2 * p) * 144 + 36 * g) * 1024;
  auto ks4 = [&](int j) { return j < 8 ? 8 * ((s + (j >> 1)) & 3) + 2 * (j & 1) : j < 16 ? 8 * ((s + j) & 3) + (j >> 2) * 2 : 2 * j; };
  unsigned ta[8], tz = L + 73728 + p * 2048 + vz, t4e, fv[4];
  u32x4 fc;
  ta[0] = L + row * 256 + (((4 * tf) ^ (row & 7)) * 32) + (c & 3) * 8;
  sfor<4>([&](auto I) { ta[I] = ta[0] ^ (I * 32); pin(ta[I]); }); pin(tz);
  bf16x8 Z[18][2], A[4], B[16][2];
  u32x2 Bs[2][2][2];
  f32x4 acc[9][2], ac4[9][2];
  auto rd_ = [&](auto I) {
    constexpr int i = I, wi = i >> 2, ph = phys(endidx(wi) - 1), hi = ph >= 9, so = (ph % 9) * 8192;
    if constexpr (i == 72) sfor<4>([&](auto K) { ta[K] = ((L + row * 128 + (((2 * tf) ^ ((row >> 1) & 3)) * 32) + (c & 3) * 8) ^ ((K & 1) * 32)) + (K >> 1) * 73728; pin(ta[K]); });
    if constexpr (i == 144) sfor<2>([&](auto K) { ta[K] = L + row * 64 + ((tf ^ ((row >> 2) & 1)) * 32) + (c & 3) * 8 + K * 73728; pin(ta[K]); });
    if constexpr (i == 162) { sfor<8>([&](auto K) { ta[K] = (L + tf * 73728 + row * 256 + (((row & 7) ^ (2 * g)) * 32) + (c & 3) * 8) ^ (K * 32); pin(ta[K]); }); t4e = L + L_SP + tf * 1024 + row * 32 + (c & 3) * 8; pin(t4e); }
    if constexpr (i < 72) {
      if constexpr ((i & 3) == 0) sfor<2>([&](auto K) { Z[wi][K] = wi < 16 ? ldsfrag(tz + so + K * 1024) : splitfrag(tz - lane * 8 + so + K * 1024); });
      A[i & 3] = trfrag(ta[i & 3] + so, 4096);
    } else if constexpr (i < 144) A[i & 3] = trfrag(ta[2 * hi + (i & 1)] + so + ((i >> 1) & 1) * 4096, 2048);
    else if constexpr (i < 162) A[i & 3] = trfrag(ta[hi] + so + (i & 3) * 2048, 1024);
    else { constexpr int j = (i - 162) / 9, u = (i - 162) % 9; if constexpr (u < 8) A[i & 3] = trfrag(ta[u] + ((3 + j) & 7) * 8192, 4096); else A[i & 3] = trfrag(t4e + (j & 7) * 2048, 512); }
  };
  TS(11);
  sfor<4>(rd_);
  float ca[2], cb[2], sg[2], gp;
  unsigned pk[4];
  auto gl = [&](auto V) {
    constexpr int v = V, pr = v >> 4, t2 = v < 64 ? (v >> 3) & 1 : (v >> 2) & 1, tl = v < 64 ? 2 * pr + ((v >> 2) & 1) : 8;
    const float o = gelu(acc[tl][t2][v & 3], ca[t2], cb[t2], sg[t2]);
    if constexpr (v & 1) pk[(v >> 1) & 3] = pk2(gp, o); else gp = o;
    if constexpr (v < 64 && (v & 7) == 7) st16c(hb + (t2 * 144 + 8 * s + (pr < 2 ? 2 * tf + pr : 2 * pr + tf)) * 1024, vz, u32x4{pk[0], pk[1], pk[2], pk[3]});
    if constexpr (v >= 64 && (v & 3) == 3) st8c(hb + (t2 * 144 + 32 + s) * 1024 + tf * 512, lane * 8, u32x2{pk[2 * t2], pk[2 * t2 + 1]});
  };
  const int fr = 8 * T + 2 * p + tf, t = 16 * fr + c, n0 = 128 * s + 32 * g + 4 * q, ne = 512 + 16 * s + 4 * q;
  unsigned yv[12], mu; u32x2 xf, st, ce, pe[3]; u32x4 xm, cm, pm[3];
  unsigned xo, xo2;
  auto finld = [&](auto I) {
    if constexpr (I < 12) ld4c<0>(yv[I], ws + O_YT + ((I & 3) + (I >> 2 == 1 ? 16 : 0)) * 4096, I < 8 ? n0 * 4096 + (fr >> 5) * 1024 + (fr & 15) * 64 + (fr & 16) / 2 + (c >> 2) * 16 + (c & 2) * 2 : ne * 4096 + (t >> 1) * 4);
    else if constexpr (I == 12) { unsigned l = vz; pin(l); xo = (16 * fr + (l >> 6)) * 1152; xo2 = xo + (l & 48) / 2; xo += l & 48; ld16<0>(xm, x + 256 * s + 64 * g, xo); }
    else if constexpr (I == 13) ld8<1024>(xf, x + 32 * s, xo2);
    else if constexpr (I == 15) ld8c<0>(st, ws + O_XST, t * 8);
    else if constexpr (I == 16) ld4c<0>(mu, ws + O_MU, t * 4);
    else if constexpr (I == 14) ld16c(cm, ws + O_XC + (size_t)(fr * 18 + 4 * s + g) * 1024, vz);
    else ld8c<0>(ce, ws + O_XC + (size_t)(fr * 36 + 32 + s) * 512, lane * 8);
  };
  auto step = [&](auto I) {
    constexpr int i = I, j = (i - 162) / 9, u = (i - 162) % 9;
    if constexpr (i < 162 ? (i + 4) % 8 == 0 : (u == 5 && j < 17)) {
      waitv<vcnt(i)>(); bar();
      if constexpr (i >= 156) { constexpr int jn = i < 162 ? 0 : j + 1; if constexpr (jn < 16) { pin(B[jn][0]); pin(B[jn][1]); } else sfor<4>([&](auto K) { pin(Bs[jn - 16][K >> 1][K & 1]); }); }
      if constexpr (i == PUB[0]) { if (tid == 64) st4c(fl + FC, b * 4, tag); }
      sfor<4>([&](auto E) {
        if constexpr (PUB[E] == i) { if (tid == 0) st4c(fl + F3 + E * 2048, fid, tag); }
        if constexpr (EXM[E] == i) { pin(fv[E]); if (!allq(fv[E], tag)) spin<unsigned>(fl + F3 + E * 2048, f3o, tag); }
      });
      if constexpr (EXM[4] == i) { pin(fc); if (!allq(fc, tag)) spin<u32x4>(fl + FC, vz, tag); }
    }
    const f32x4 z4 = {};
    if constexpr (i < 162) {
      constexpr int ks = i < 72 ? i >> 2 : i < 108 ? (i - 72) >> 1 : i < 144 ? (i - 108) >> 1 : i - 144;
      constexpr int tl = i < 72 ? i & 3 : i < 108 ? 4 + (i & 1) : i < 144 ? 6 + (i & 1) : 8;
      sfor<2>([&](auto K) { acc[tl][K] = mfma(A[i & 3], Z[ks][K], ks == 0 ? z4 : acc[tl][K]); });
    } else sfor<2>([&](auto K) {
      bf16x8 bf;
      if constexpr (j < 16) bf = B[j][K]; else bf = __builtin_bit_cast(bf16x8, __builtin_shufflevector(Bs[j - 16][K][0], Bs[j - 16][K][1], 0, 1, 2, 3));
      ac4[u][K] = mfma(A[i & 3], bf, j == 0 ? z4 : ac4[u][K]);
    });
    if constexpr (i + 4 < 324) rd_(IC<i + 4>{});
    if constexpr (i >= 4 && i < 162) { constexpr int G = (i + 4) / 8, k = (i + 4) % 8; if constexpr ((k & 1) && lim3(G - 1) + (k >> 1) < lim3(G)) issue(IC<lim3(G - 1) + (k >> 1)>{}); }
    sfor<36>([&](auto N) {
      constexpr int jj = N >> 1, k = N & 1;
      if constexpr (wIt(jj, k) == i) dma(L + (((3 + jj) & 7) + 9 * k) * 8192 + w * 1024, wk + 256 * s + (ks4(jj) + k) * 36864, vo);
      if constexpr (bIt(jj, k) == i) {
        const char* hp = hb + ((k ^ tf) * 144 + ks4(jj) + tf) * 1024;
        if constexpr (jj < 16) ld16c(B[jj][k], hp, vz); else { ld8c<0>(Bs[jj - 16][k][0], hp, lane * 8); ld8c<512>(Bs[jj - 16][k][1], hp, lane * 8); }
      }
    });
    sfor<5>([&](auto E) {
      if constexpr (eIt(E) == i) dma(L + L_SP + (E & 1) * 8192 + w * 1024, wk + 1024 + 32 * s + (ks4(E < 4 ? 4 * E + (w >> 1) : 16 + (p >> 1)) + (w & 1)) * 36864, ve);
      if constexpr (LDF[E] == i) { if constexpr (E < 4) ld4c<0>(fv[E], fl + F3 + E * 2048, f3o); else ld16c(fc, fl + FC, vz); }
    });
    if constexpr (i >= FIN && i < FIN + 18) finld(IC<i - FIN>{});
    if constexpr (i == PF4) { if (w >= 2) pf(L, wk + xi * 41472, min((int)vz * 8 + (w - 2) * 8192, 323 * 128)); }
    if constexpr (i >= CLR && i <= CLR + 12 && (i - CLR) % 4 == 0) {
      const u32x4 n4 = {~0u, ~0u, ~0u, ~0u};
      if constexpr (i < CLR + 12) st16c(ws + O_P + (size_t)((b * 3 + (i - CLR) / 4) * 8 + w) * 1024, vz, n4);
      else if (w < 3) st16c(ws + O_PE + (size_t)(b * 3 + w) * 1024, vz, n4);
    }
    if constexpr (i == 36) TX(12);
    if constexpr (i == 72) TX(13);
    if constexpr (i == 108) TX(14);
    if constexpr (i == 144) TX(15);
    if constexpr (i == 162) TS(16);
    if constexpr (i == 216) TX(17);
    if constexpr (i == 270) TX(18);
    if constexpr (i == 60) sfor<2>([&](auto J) {
      float y = 0;
      sfor<4>([&](auto K) { pin(sq[4 * J + K]); y += __builtin_bit_cast(float, sq[4 * J + K]); });
      const float vr = sumq(y) * (1.f / D_) + EPS, r = __builtin_amdgcn_rsqf(vr); sg[J] = vr * r; ca[J] = -GK * r; cb[J] = -GK * GC * r * r * r;
    });
    if constexpr (gV(i) >= 0) gl(IC<gV(i)>{});
    SB();
  };
  sfor<162>(step);
  sfor<162>([&](auto I) { step(IC<162 + I>{}); });
  TS(19);
  bar();
  sfor<9>([&](auto U) { *(LDS3 f32x4*)(L + w * 9216 + U * 1024 + vz) = ac4[U][1]; });
  barl();
  f32x4 z[9];
  sfor<9>([&](auto U) { z[U] = ac4[U][0] + *(LDS3 f32x4*)(L + (w ^ 4) * 9216 + U * 1024 + vz); });
  auto pz = [&](float a, float d) { const unsigned u = pk2(a, d); return u < 0xfffffffeu ? u : 0xfffffffeu; };
  sfor<3>([&](auto R) { constexpr int r = R + 1; st16c(ws + O_P + (size_t)(((b ^ r) * 3 + R) * 8 + w) * 1024, vz, u32x4{pz(z[2 * r][0], z[2 * r][1]), pz(z[2 * r][2], z[2 * r][3]), pz(z[2 * r + 1][0], z[2 * r + 1][1]), pz(z[2 * r + 1][2], z[2 * r + 1][3])}); });
  const bool own = p == g;
  if (!own) st8c(ws + O_PE + (size_t)(((b ^ p ^ g) * 3 + (p ^ g) - 1) * 2 + tf) * 512, lane * 8, u32x2{pz(z[8][0], z[8][1]), pz(z[8][2], z[8][3])});
  TS(20);
  waitv<3>(); pin(xm); pin(xf);
  const unsigned lt = L + 81920 + w * 2048, lq = lt + c * 64 + q * 8;
  *(LDS3 u32x4*)(lt + vz) = xm; *(LDS3 u32x2*)(lt + 1024 + lane * 8) = xf;
  u32x2 xa[3] = {*(LDS3 u32x2*)lq, *(LDS3 u32x2*)(lq + 32), *(LDS3 u32x2*)(lt + 1024 + c * 32 + q * 8)};
  sfor<3>([&](auto I) { pin(xa[I]); });
#pragma unroll 1
  for (int it = 0; it < (1 << 17); ++it) {
    asm volatile("global_load_dwordx4 %0, %6, %8 sc1\n\tglobal_load_dwordx4 %1, %6, %9 sc1\n\tglobal_load_dwordx4 %2, %6, %10 sc1\n\t"
                 "global_load_dwordx2 %3, %7, %11 sc1\n\tglobal_load_dwordx2 %4, %7, %11 offset:1024 sc1\n\tglobal_load_dwordx2 %5, %7, %11 offset:2048 sc1\n\ts_waitcnt vmcnt(0)"
                 : "=&v"(pm[0]), "=&v"(pm[1]), "=&v"(pm[2]), "=&v"(pe[0]), "=&v"(pe[1]), "=&v"(pe[2])
                 : "v"(vz), "v"(lane * 8), "s"(ws + O_P + (size_t)(b * 24 + w) * 1024), "s"(ws + O_P + (size_t)(b * 24 + 8 + w) * 1024), "s"(ws + O_P + (size_t)(b * 24 + 16 + w) * 1024), "s"(ws + O_PE + (size_t)(b * 6 + tf) * 512) : "memory");
    unsigned mx = 0;
    sfor<12>([&](auto I) { const unsigned u = pm[I >> 2][I & 3]; mx = u > mx ? u : mx; });
    sfor<6>([&](auto I) { const unsigned u = pe[I >> 1][I & 1]; mx = u > mx ? u : mx; });
    if (__builtin_amdgcn_ballot_w64(mx == ~0u) == 0) break;
  }
  TS(22);
  sfor<12>([&](auto I) { pin(yv[I]); }); pin(st); pin(mu); pin(cm); pin(ce);
  const f32x2 sf = __builtin_bit_cast(f32x2, st);
  auto fin = [&](auto H) {
    float r[4];
    sfor<4>([&](auto E) {
      constexpr int e = E, k = 2 * H + (e >> 1);
      auto X = [&](unsigned u) { return (e & 1) ? bhi(u) : blo(u); };
      float ob = __builtin_bit_cast(float, mu) + z[H < 2 ? H : 8][e];
      if constexpr (H < 2) ob += (X(cm[k]) + X(pm[0][k])) + (X(pm[1][k]) + X(pm[2][k])); else ob += (X(ce[k - 4]) + X(pe[0][k - 4])) + (X(pe[1][k - 4]) + X(pe[2][k - 4]));
      const float xf = X(xa[H][e >> 1]), y = (c & 1) ? bhi(yv[4 * H + e]) : blo(yv[4 * H + e]);
      r[e] = xf + (xf - sf[0]) * sf[1] * y * (ob * __builtin_amdgcn_rcpf(1.f + __builtin_amdgcn_exp2f(-1.44269504f * ob)));
    });
    *(LDS3 u32x2*)(H < 2 ? lq + 32 * H : lt + 1024 + c * 32 + q * 8) = u32x2{pk2(r[0], r[1]), pk2(r[2], r[3])};
  };
  fin(IC<0>{}); fin(IC<1>{});
  *(u32x4*)(out + 256 * s + 64 * g + xo) = *(LDS3 u32x4*)(lt + vz);
  if (own) { fin(IC<2>{}); *(u32x2*)(out + 1024 + 32 * s + xo2) = *(LDS3 u32x2*)(lt + 1024 + lane * 8); }
  TS(23);
  TSEND;
}

extern "C" void kernel_launch(void* const* d_in, const int* in_sizes, int n_in, void* d_out, int out_size, void* d_ws, size_t ws_size, hipStream_t stream) {
  k_all<<<256, 512, 0, stream>>>((const char*)d_in[0], (const char*)d_in[1], (const char*)d_in[2], (const char*)d_in[3], (const char*)d_in[4], (const unsigned*)d_in[5],
                                 (const char*)d_in[7], (const char*)d_in[8], (const char*)d_in[9], (const char*)d_in[10], (char*)d_ws, (char*)d_out);
}
```

```cpp
#include <hip/hip_runtime.h>
#include <hip/hip_bf16.h>
#include <utility>

typedef __attribute__((ext_vector_type(2))) __bf16 bf16x2;
typedef __attribute__((ext_vector_type(4))) __bf16 bf16x4;
typedef __attribute__((ext_vector_type(8))) __bf16 bf16x8;
typedef __attribute__((ext_vector_type(2))) float f32x2;
typedef __attribute__((ext_vector_type(4))) float f32x4;
typedef __attribute__((ext_vector_type(2))) unsigned u32x2;
typedef __attribute__((ext_vector_type(4))) unsigned u32x4;
#define LDS3 __attribute__((address_space(3)))
#define DEV __device__ __forceinline__
#define SB() __builtin_amdgcn_sched_barrier(0)

constexpr int S_ = 2048, D_ = 576, F_ = 4608;
constexpr float EPS = 1e-5f;
constexpr float GK = 2.302208198f;
constexpr float GC = 0.044715f;
constexpr size_t SZ_ACT = (size_t)S_ * D_ * 2;
constexpr size_t O_HS = 0, O_XC = O_HS + SZ_ACT, O_KVT = O_XC + SZ_ACT, O_YT = O_KVT + SZ_ACT,
                 O_P = O_YT + SZ_ACT, O_H = O_P + 4 * SZ_ACT, O_XST = O_H + 8 * SZ_ACT,
                 O_MU = O_XST + 16384, O_SS = O_MU + 8192, O_RM = O_SS + 131072, O_E = O_RM + 2048, O_F = O_E + 1024, O_PE = O_P + 3 * SZ_ACT;
constexpr int NR = 1, RS = 4352, FK = 16384, FC = FK + 2048, F2 = 2048, F3 = 4096, F1 = F3 + 4 * 2048;

template <class F, int... I> DEV void sfor_(F&& f, std::integer_sequence<int, I...>) { (f(std::integral_constant<int, I>{}), ...); }
template <int N, class F> DEV void sfor(F&& f) { sfor_(f, std::make_integer_sequence<int, N>{}); }

DEV void dma(unsigned lds, const char* g, unsigned voff) {
  asm volatile("s_mov_b32 m0, %0\n\ts_nop 0\n\tglobal_load_lds_dwordx4 %1, %2" ::"s"(lds), "v"(voff), "s"(g) : "memory");
}
template <int N> DEV void waitv() { asm volatile("s_waitcnt vmcnt(%0)" ::"n"(N) : "memory"); }
DEV void bar() { asm volatile("s_barrier" ::: "memory"); }
DEV void barl() { asm volatile("s_waitcnt lgkmcnt(0)\n\ts_barrier" ::: "memory"); }
template <int O> DEV void ld8(u32x2& d, const char* g, unsigned voff) { asm volatile("global_load_dwordx2 %0, %1, %2 offset:%3" : "=v"(d) : "v"(voff), "s"(g), "n"(O) : "memory"); }
template <int O> DEV void ld16(u32x4& d, const char* g, unsigned voff) { asm volatile("global_load_dwordx4 %0, %1, %2 offset:%3" : "=v"(d) : "v"(voff), "s"(g), "n"(O) : "memory"); }
template <int N, class T> DEV void syncb(T& a, T& b) { asm volatile("s_waitcnt vmcnt(%2)\n\ts_barrier" : "+v"(a), "+v"(b) : "n"(N) : "memory"); }
#define TS(i)
#define TSDECL
#define TSEND
DEV void dmac(unsigned lds, const char* g, unsigned voff) {
  asm volatile("s_mov_b32 m0, %0\n\ts_nop 0\n\tglobal_load_lds_dwordx4 %1, %2 sc1" ::"s"(lds), "v"(voff), "s"(g) : "memory");
}
template <int O> DEV void ld4(unsigned& d, const char* g, unsigned voff) { asm volatile("global_load_dword %0, %1, %2 offset:%3" : "=v"(d) : "v"(voff), "s"(g), "n"(O) : "memory"); }
template <int O> DEV void ld4c(unsigned& d, const char* g, unsigned voff) { asm volatile("global_load_dword %0, %1, %2 offset:%3 sc1" : "=v"(d) : "v"(voff), "s"(g), "n"(O) : "memory"); }
template <int O> DEV void ld8c(u32x2& d, const char* g, unsigned voff) { asm volatile("global_load_dwordx2 %0, %1, %2 offset:%3 sc1" : "=v"(d) : "v"(voff), "s"(g), "n"(O) : "memory"); }
template <class T> DEV void ld16c(T& d, const char* g, unsigned voff) { asm volatile("global_load_dwordx4 %0, %1, %2 sc1" : "=v"(d) : "v"(voff), "s"(g) : "memory"); }
DEV void st4c(const char* g, unsigned voff, unsigned d) { asm volatile("global_store_dword %0, %1, %2 sc1" ::"v"(voff), "v"(d), "s"(g) : "memory"); }
DEV void st8c(const char* g, unsigned voff, u32x2 d) { asm volatile("global_store_dwordx2 %0, %1, %2 sc1" ::"v"(voff), "v"(d), "s"(g) : "memory"); }
template <int O = 0> DEV void st16c(const char* g, unsigned voff, u32x4 d) { asm volatile("global_store_dwordx4 %0, %1, %2 offset:%3 sc1\n\ts_nop 1" ::"v"(voff), "v"(d), "s"(g), "n"(O) : "memory"); }
DEV bool allq(unsigned v, unsigned tag) { return __builtin_amdgcn_ballot_w64(v != tag) == 0; }
DEV bool allq(u32x4 v, unsigned tag) { return __builtin_amdgcn_ballot_w64(((v[0] ^ tag) | (v[1] ^ tag) | (v[2] ^ tag) | (v[3] ^ tag)) != 0) == 0; }
template <class T> DEV void spin(const char* g, unsigned voff, unsigned tag) {
#pragma unroll 1
  for (int it = 0; it < (1 << 17); ++it) {
    T v;
    if constexpr (sizeof(T) == 4) asm volatile("global_load_dword %0, %1, %2 sc1\n\ts_waitcnt vmcnt(0)" : "=&v"(v) : "v"(voff), "s"(g) : "memory");
    else asm volatile("global_load_dwordx4 %0, %1, %2 sc1\n\ts_waitcnt vmcnt(0)" : "=&v"(v) : "v"(voff), "s"(g) : "memory");
    if (allq(v, tag)) break;
    __builtin_amdgcn_s_sleep(2);
  }
}
template <class T> DEV void pin(T& d) { asm volatile("" : "+v"(d)); }

DEV bf16x8 trfrag(unsigned a, int second) {
  bf16x4 x = __builtin_amdgcn_ds_read_tr16_b64_v4bf16((LDS3 bf16x4*)a);
  bf16x4 y = __builtin_amdgcn_ds_read_tr16_b64_v4bf16((LDS3 bf16x4*)(a + second));
  return __builtin_shufflevector(x, y, 0, 1, 2, 3, 4, 5, 6, 7);
}
DEV bf16x8 ldsfrag(unsigned a) { return *(LDS3 bf16x8*)a; }
DEV f32x4 mfma(bf16x8 a, bf16x8 b, f32x4 c) { return __builtin_amdgcn_mfma_f32_16x16x32_bf16(a, b, c, 0, 0, 0); }
DEV unsigned pk2(float a, float b) { f32x2 v = {a, b}; return __builtin_bit_cast(unsigned, __builtin_convertvector(v, bf16x2)); }
DEV float blo(unsigned u) { return __builtin_bit_cast(float, u << 16); }
DEV float bhi(unsigned u) { return __builtin_bit_cast(float, u & 0xffff0000u); }
DEV float gelu(float u, float ca, float cb, float sg) {
  float t = u * u; float p = __builtin_fmaf(t, cb, ca); float e = __builtin_amdgcn_exp2f(u * p);
  float o = u * __builtin_amdgcn_rcpf(__builtin_fmaf(e, sg, sg)); pin(o); return o;
}
template <int C> DEV float dpp(float v) { return __builtin_bit_cast(float, __builtin_amdgcn_update_dpp(0, __builtin_bit_cast(int, v), C, 0xf, 0xf, true)); }
DEV float sum16(float v) { v += dpp<0x128>(v); v += dpp<0x124>(v); v += dpp<0x122>(v); v += dpp<0x121>(v); return v; }
DEV float sum8(float v) { v += dpp<0xB1>(v); v += dpp<0x4E>(v); v += dpp<0x141>(v); return v; }
DEV float dsum(unsigned u, float a) { return __builtin_amdgcn_fdot2_f32_bf16(__builtin_bit_cast(bf16x2, u), __builtin_bit_cast(bf16x2, 0x3f803f80u), a, false); }
DEV float sumq(float v) { v += __shfl_xor(v, 16); v += __shfl_xor(v, 32); return v; }

DEV bf16x8 splitfrag(unsigned a) { const u32x2 x = *(LDS3 u32x2*)a, y = *(LDS3 u32x2*)(a + 512); return __builtin_bit_cast(bf16x8, u32x4{x[0], x[1], y[0], y[1]}); }
template <int I> using IC = std::integral_constant<int, I>;
#define TX(i)

constexpr int L_CV = 131072, CV_SZ = 8736, CV_U = 512, CV_H = 4640, L_EXT = 100352, L_ACT = 118784, L_HS = 73728, L_EX2 = 110592, L_RM = 129024, L_RB = 98304, L_SP = 147456, L_SC = 98304, L_JK = 163584;
DEV void pf(unsigned L, const char* g, unsigned voff) { asm volatile("s_mov_b32 m0, %0\n\ts_nop 0\n\tglobal_load_lds_dword %1, %2" ::"s"(L + L_JK), "v"(voff), "s"(g) : "memory"); }
DEV bf16x8 toep(unsigned a, unsigned sh) {
  const LDS3 unsigned* hp = (const LDS3 unsigned*)a;
  const unsigned d0 = hp[0], d1 = hp[1], d2 = hp[2], d3 = hp[3], d4 = hp[4];
  return __builtin_bit_cast(bf16x8, u32x4{__builtin_amdgcn_alignbyte(d1, d0, sh), __builtin_amdgcn_alignbyte(d2, d1, sh), __builtin_amdgcn_alignbyte(d3, d2, sh), __builtin_amdgcn_alignbyte(d4, d3, sh)});
}
constexpr int imin(int a, int b) { return a < b ? a : b; }
constexpr int imax(int a, int b) { return a > b ? a : b; }
constexpr int endidx(int wi) { return wi < 0 ? 0 : wi < 18 ? 2 * wi + 2 : wi > 40 ? 59 : 19 + wi; }
constexpr int lim3(int G) { return imin(59, 18 + endidx(2 * G - 2)); }
constexpr int phys(int n) { return ((n % 18) & 1) ? (n % 18) >> 1 : 9 + ((n % 18) >> 1); }
constexpr int slotIt(int n) { int G = 1; while (lim3(G) <= n) ++G; return 8 * G - 3 + 2 * (n - lim3(G - 1)); }
constexpr int PUB[4] = {124, 176, 212, 230}, LDF[5] = {143, 190, 226, 250, 216}, EXM[5] = {156, 203, 239, 266, 239};
constexpr int CODE = 56160, FIN = 284, CLR = 42, PF4 = -1, PB3 = 0, EF = 11, W2L = 1, P4C = 11, DLY = 1, ZE = 0, PRI = 0;
constexpr int gI(int v) { return v < 32 ? 74 + v * 4 / 3 : v < 48 ? 118 + (v - 32) * 11 / 4 : 162 + (v - 48) * 5 / 2; }
constexpr int gV(int n) { for (int v = 0; v < 72; ++v) if (gI(v) == n) return v; return -1; }
constexpr int wIt(int j, int k) { return j < 5 ? 109 + 8 * j + 2 * k : 162 + 9 * (j - 5) + 2 + k; }
constexpr int bIt(int j, int k) { return j < 2 ? PUB[0] + 2 + 2 * j + k : j < 4 ? EXM[0] - 3 + 2 * j + k : 162 + 9 * (j - 4) + k; }
constexpr int eIt(int e) { return e < 2 ? 101 + 30 * e : 162 + 9 * (4 * e - 4) + 6; }
constexpr int nops(int n) {
  if (n < 0) return n == -2 ? 8 : 1;
  int c = 0;
  for (int i = 18; i < 59; ++i) c += slotIt(i) == n;
  for (int j = 0; j < 18; ++j) for (int k = 0; k < 2; ++k) c += (wIt(j, k) == n) + (bIt(j, k) == n) * (j < 16 ? 1 : 2);
  for (int e = 0; e < 5; ++e) c += (eIt(e) == n) + (LDF[e] == n);
  c += (n >= FIN && n < FIN + 18) + (n == CLR || n == CLR + 4 || n == CLR + 8);
  const int v = gV(n);
  return c + (v >= 0 && (v < 64 ? (v & 7) == 7 : (v & 3) == 3));
}
constexpr int need4(int j) { return imax(imax(wIt(j, 1), bIt(j, 1)), eIt(j / 4)); }
constexpr int vcnt(int n) {
  int l = 0;
  if (n < 162) { const int e = endidx(2 * ((n + 4) / 8) + 1); l = e <= 18 ? -12 + e / 2 : slotIt(e - 1); if (n == 156) l = imax(l, need4(0)); }
  else l = need4((n - 162) / 9 + 1);
  for (int e = 0; e < 5; ++e) { if (e < 4 && PUB[e] == n) l = imax(l, gI(e < 3 ? 31 + 16 * e : 71)); if (EXM[e] == n) l = imax(l, LDF[e]); }
  int c = 0;
  for (int m = l + 1; m < n; ++m) c += nops(m);
  return c;
}

__global__ __launch_bounds__(512, 2) void k_all(const char* __restrict__ x, const char* __restrict__ kk, const char* __restrict__ vv, const char* __restrict__ shk,
    const char* __restrict__ ssk, const unsigned* __restrict__ ssd, const char* __restrict__ w1, const char* __restrict__ w2, const char* __restrict__ w3,
    const char* __restrict__ w4, char* __restrict__ ws, char* __restrict__ out) {
  __shared__ __attribute__((aligned(1024))) char smem[163840];
  const unsigned L = (unsigned)(size_t)(LDS3 char*)smem;
  const int b = blockIdx.x, tid = threadIdx.x, lane = tid & 63, w = __builtin_amdgcn_readfirstlane(tid >> 6);
  const int g = b & 3, T = ((b >> 2) & 1) * 8 + (b >> 5), s = (b >> 3) & 3, T32 = 4 * T + g, g2 = s, xi = b >> 3;
  const int q = lane >> 4, c = lane & 15, tf = w >> 2, p = w & 3, row = 4 * q + (c >> 2);
  const char* fl = ws + O_F;
  const int fid = (T * 32 + g * 4 + s) * 4, ch3 = 512 + (b >> 2), chA = 8 * (b >> 2) + g;
  TSDECL;
  if (w == 7) { const char* pc; asm volatile("s_getpc_b64 %0" : "=s"(pc)); pf(L, pc, min(((xi & 7) * 64 + lane) * 128, CODE)); }
  if (w == 5) sfor<3>([&](auto I) { pf(L, w1 + 20736 * xi, min(lane + 64 * I, 161) * 128); });
  unsigned tag;
  const unsigned vz = lane * 16;
  const bool ext = p == tf;
  const int rr = 4 * w + q;
  const unsigned vo = rr * 1152 + ((c ^ ((rr & 7) << 1)) * 16), ve = (lane >> 1) * 1152 + (lane & 1) * 16;
  const unsigned t0 = L + row * 256 + (((2 * p) ^ (row & 7)) * 32) + (c & 3) * 8, t1 = t0 ^ 32, te = L + L_EXT + row * 32 + (c & 3) * 8;
  const char* wm2 = w2 + 256 * g2;
  {
    const char* wm = w1 + 288 * g2 + 32 * (g2 & 1);
    const char* we = w1 + 288 * g2 + ((g2 & 1) ? 0 : 256);
    u32x2 ar[19]; u32x4 sk[2];
    const int kb = 36 * g2 + 12 * p, kd = kb + min(c, 11), kt = 32 * T32 + 8 * q;
    const int rmr = (q < 2 || b >= 32) ? 2 * b + (q & 1) : 510 + 2 * b + q;
    const int r = tid >> 3, e = tid & 7, srow = 32 * T32 + r + ((g2 & 1) ? 1 : -1) * (1 + (g2 >> 1));
    const bool ok = srow >= 0 && srow < S_;
    f32x2 xs = {};
    unsigned xc[36];
    if (w < 4) {
      u32x4 xv[9];
      { const unsigned xo = (unsigned)(ok ? srow : 0) * 1152 + e * 16; sfor<9>([&](auto I) { ld16<I * 128>(xv[I], x, xo); }); }
      ld4c<0>(tag, ws + O_E, 0u);
      const unsigned za = L + L_ACT + r * 1184 + (e >> 2) * 64 + (e & 1) * 32 + (e & 2) * 4;
      bar(); waitv<0>();
      TS(1);
      sfor<9>([&](auto I) { pin(xv[I]); }); pin(tag);
      float sm[4] = {};
      sfor<36>([&](auto I) { sm[I & 3] = dsum(xv[I >> 2][I & 3], sm[I & 3]); });
      const float m = sum8((sm[0] + sm[1]) + (sm[2] + sm[3])) * (1.f / D_);
      sfor<36>([&](auto I) { const unsigned u = xv[I >> 2][I & 3]; xc[I] = pk2(blo(u) - m, bhi(u) - m); if constexpr (I & 1) *(LDS3 u32x2*)(za + (I >> 2) * 128 + (I & 2) * 8) = u32x2{xc[I - 1], xc[I]}; });
      xs[0] = m;
    } else {
      ld4c<0>(tag, ws + O_E, 0u);
      bar();
      sfor<12>([&](auto N) {
        dma(L + N * 8192 + p * 1024, wm + (size_t)N * 32 * 1152, vo - 18432); dma(L + N * 8192 + w * 1024, wm + (size_t)N * 32 * 1152, vo);
        if (p == (N & 3)) dma(L + L_EXT + N * 1024, we + (size_t)N * 32 * 1152, ve);
      });
      TS(13);
      waitv<24>(); pin(tag);
    }
    const bool kw = w < 7;
    const char* b0 = kw ? kk : w2;
    const int o0 = kw ? (kt - 3) * 1152 + kd * 8 : rmr * 1152 + c * 8, s0 = kw ? 1152 : 128, m0 = kw ? 1 << 30 : o0 + 1024, o1 = kw ? o0 + 3456 : 0;
    tag += 1;
    f32x4 a0 = {}, a1 = {}, ae = {};
    const unsigned bb = L + L_ACT + (16 * tf + c) * 1184 + q * 16;
    TS(2);
    bf16x8 A0[6], A1[6];
    auto rdw = [&](auto K) { sfor<6>([&](auto I) { constexpr int so = ((K + I) % 12) * 8192; A0[I] = trfrag(t0 + so, 4096); A1[I] = trfrag(t1 + so, 4096); }); };
    auto mm = [&](auto K) {
      bf16x8 B[6];
      sfor<6>([&](auto I) { B[I] = ldsfrag(bb + (K + I) * 64); });
      if (ext) { bf16x8 E[6]; sfor<6>([&](auto I) { E[I] = trfrag(te + (K + I) * 1024, 512); }); sfor<6>([&](auto I) { ae = mfma(E[I], B[I], ae); }); }
      sfor<6>([&](auto I) { a0 = mfma(A0[I], B[I], a0); a1 = mfma(A1[I], B[I], a1); });
    };
    waitv<13>(); barl();
    TS(15);
    rdw(IC<0>{});
    waitv<0>(); barl();
    sfor<6>([&](auto I) { dma(L + I * 8192 + w * 1024, wm + (size_t)(12 + I) * 32 * 1152, vo); if (w == I) dma(L + L_EXT + (12 + I) * 1024, we + (size_t)(12 + I) * 32 * 1152, ve); });
    if (!tf) pf(L, w2 + 20736 * xi, min(tid, 161) * 128);
    mm(IC<0>{}); rdw(IC<6>{}); mm(IC<6>{});
    TS(17);
    if (tf) {
      sfor<11>([&](auto I) { ld8<0>(ar[I], b0, min(max(o0 + I * s0, 0), m0)); });
      sfor<8>([&](auto I) { ld8<(I % 3) * 1152>(ar[11 + I], vv, o1 + (I / 3) * 3456); });
      ld16<0>(sk[0], shk, min(kd, 143) * 32); ld16<16>(sk[1], shk, min(kd, 143) * 32);
      waitv<21>();
    } else {
      float v4[4] = {};
      sfor<36>([&](auto I) { const bf16x2 y = __builtin_bit_cast(bf16x2, xc[I]); v4[I & 3] = __builtin_amdgcn_fdot2_f32_bf16(y, y, v4[I & 3], false); });
      const float v = sum8((v4[0] + v4[1]) + (v4[2] + v4[3])) * (1.f / D_), rs = __builtin_amdgcn_rsqf(v + EPS);
      *(LDS3 float*)(L + L_SC + r * 4) = ok ? rs * __builtin_amdgcn_rsqf(v * rs * rs + EPS) : 0.f;
      xs[1] = rs;
      waitv<1>();
    }
    barl();
    TS(18);
    rdw(IC<12>{});
    const float sc = *(LDS3 float*)(L + L_SC + (16 * tf + c) * 4);
    if constexpr (!W2L) sfor<6>([&](auto I) { dma(L + (6 + I) * 8192 + w * 1024, wm2 + (size_t)I * 32 * 1152, vo); });
    mm(IC<12>{});
    TS(3);
    if (tf) {
      waitv<W2L ? 0 : 6>();
      TS(21);
      sfor<19>([&](auto I) { pin(ar[I]); }); pin(sk[0]); pin(sk[1]);
      if (kw) {
        const int l2 = min(lane, 47), sl = ((l2 & 3) * 16 + (l2 >> 2)) * 4;
        if (kt == 0) ar[0] = ar[1] = ar[2] = u32x2{0, 0};
        sfor<2>([&](auto H) {
          auto X = [&](const u32x2& u) { const unsigned y = u[(int)H]; return f32x2{blo(y), bhi(y)}; };
          const u32x4 sh = sk[H];
          const f32x2 c3 = {blo(sh[0]), blo(sh[2])}, c2 = {bhi(sh[0]), bhi(sh[2])}, c1 = {blo(sh[1]), blo(sh[3])}, c0 = {bhi(sh[1]), bhi(sh[3])};
          f32x2 kf[11], r0[8];
          sfor<11>([&](auto I) { kf[I] = X(ar[I]); });
          sfor<8>([&](auto I) { r0[I] = (kf[I + 3] * c3 + kf[I + 2] * c2 + kf[I + 1] * c1 + kf[I] * c0) * X(ar[11 + I]); });
          sfor<2>([&](auto E) {
            auto P = [&](f32x2 y0, f32x2 y1) { return (unsigned)__builtin_amdgcn_ds_bpermute(sl, (int)pk2(y0[(int)E], y1[(int)E])); };
            st16c<0>(ws + O_KVT + (2 * H + E) * 9216 + T32 * 36864 + kb * 64, l2 * 16, u32x4{P(r0[7], r0[6]), P(r0[5], r0[4]), P(r0[3], r0[2]), P(r0[1], r0[0])});
          });
        });
      } else {
        float z = 0;
        sfor<18>([&](auto I) { z = dsum(ar[I >> 1][I & 1], z); });
        z = sum16(z) * (1.f / D_);
        const float z1 = __shfl_xor(z, 16);
        if ((lane & 31) == 0) { const int k = rmr & 31; st4c(ws + O_RM, ((rmr >> 5) * 32 + ((k >> 2) & 3) * 8 + (k >> 4) * 4 + (k & 3)) * 2, pk2(z, z1)); }
      }
      TS(12);
    }
    const float ga = -GK * sc, gb = -GK * GC * sc * sc * sc, gs = __builtin_amdgcn_rcpf(sc);
    auto G = [&](float u) { return gelu(u, ga, gb, gs); };
    const char* hs = ws + O_HS + (size_t)(2 * T32 + tf) * 18 * 1024;
    st16c(hs + (((9 * g2 + 1) >> 1) + p) * 1024, vz, u32x4{pk2(G(a0[0]), G(a0[1])), pk2(G(a0[2]), G(a0[3])), pk2(G(a1[0]), G(a1[1])), pk2(G(a1[2]), G(a1[3]))});
    if (ext) st8c(hs + ((g2 & 1) ? 9 * g2 : 9 * g2 + 8) * 512, lane * 8, u32x2{pk2(G(ae[0]), G(ae[1])), pk2(G(ae[2]), G(ae[3]))});
    if (!tf && g2 < 2 && ok && e == 0) st8c(ws + O_XST, srow * 8, __builtin_bit_cast(u32x2, xs));
    if (W2L && !tf) sfor<12>([&](auto I) { dma(L + 6 * 8192 + I * 4096 + w * 1024, wm2 + (size_t)I * 16 * 1152, vo); });
    bar();
    sfor<3>([&](auto I) { dma(L + I * 8192 + w * 1024, wm2 + (size_t)(6 + I) * 32 * 1152, vo); });
    waitv<3>(); bar();
    if (tid < NR) st4c(fl + FK, b * 4 + tid * RS, tag);
    TS(4);
  }
  const char* wa = w3 + 2304 * g + 512 * s;
  const char* wk = w4 + (size_t)1327104 * g;
  const char* zs = ws + O_XC + (size_t)(8 * T + w) * 18 * 1024;
  const unsigned f3o = (T * 32 + g * 4 + (lane & 3)) * 4;
  const unsigned va = rr * 9216 + ((c ^ ((rr & 7) << 1)) * 16);
  auto pfA = [&]() { const int i = min(tid - 64, 143); pf(L, w3 + 2304 * g, (18 * xi + (i >> 3)) * 9216 + (i & 6) * 256 + (i & 1) * 128); };
  auto pfB = [&]() { const int i = min(tid - 320, 179), r = i / 10, j = i - 10 * r; pf(L, w3 + 2304 * g, (18 * xi + r) * 9216 + (j < 8 ? (j & 6) * 256 + 256 : 2048) + (j & 1) * 128); };
  {
    const int t = 32 * T32 + 16 * tf + c;
    unsigned dd;
    auto lsk = [&]() { sfor<3>([&](auto I) { dma(L + L_CV + I * CV_SZ + CV_H + p * 1024, ssk + (size_t)(I < 2 ? chA + 4 * I : ch3) * 4096 + p * 1024, vz); }); };
    sfor<3>([&](auto I) { dma(L + (3 + I) * 8192 + w * 1024, wm2 + (size_t)(9 + I) * 32 * 1152, vo); });
    sfor<3>([&](auto I) { const int n = min(8 * I + w, 17); dma(L + L_EX2 + n * 1024, w2 + 1024 + 32 * g2 + (size_t)n * 32 * 1152, ve); });
    u32x2 st, xf; u32x4 xm;
    { const unsigned xo = (32 * T32 + 16 * tf + (lane >> 2)) * 1152; ld16<0>(xm, x + 256 * g2 + 64 * p, xo + (lane & 3) * 16); ld8<1024>(xf, x + 32 * g2, xo + (lane & 3) * 8); }
    bf16x8 A0[18], A1[18];
    auto rda = [&](auto I) { constexpr int so = (I < 9 ? (I + 6) % 12 : (I - 6) % 9) * 8192; A0[I] = trfrag(t0 + so, 4096); A1[I] = trfrag(t1 + so, 4096); };
    sfor<6>(rda);
    waitv<8>();
    barl();
    sfor<3>([&](auto I) { dma(L + (6 + I) * 8192 + w * 1024, wm2 + (size_t)(12 + I) * 32 * 1152, vo); rda(IC<6 + I>{}); });
    if (w < 3 && lane < 34) *(LDS3 u32x4*)(L + L_CV + w * CV_SZ + (lane < 32 ? lane * 16 : 4096 + lane * 16)) = u32x4{0, 0, 0, 0};
    barl();
    sfor<3>([&](auto I) { dma(L + I * 8192 + w * 1024, wm2 + (size_t)(15 + I) * 32 * 1152, vo); });
    if (w == 0) spin<u32x4>(fl + FK + (b >> 3 & (NR - 1)) * RS, vz, tag);
    else waitv<0>();
    bar();
    TS(5);
    if (b == 0 && tid == 0) st4c(ws + O_E, 0u, tag);
    ld8c<0>(st, ws + O_XST, t * 8);
    sfor<5>([&](auto I) {
      const int n = min(8 * I + w, 37);
      dmac(L + (n < 36 ? L_HS : L_RM - 36864) + n * 1024, ws + (n < 36 ? O_HS + (size_t)36864 * T32 : O_RM - 36864) + n * 1024, vz);
    });
    bar();
    if (tf) lsk(); else sfor<3>([&](auto I) {
      const int ch = I < 2 ? chA + 4 * I : ch3;
      dmac(L + L_CV + I * CV_SZ + CV_U + p * 1024, ws + O_KVT + (size_t)16 * p * 36864 + (ch & 3) * 9216 + (ch >> 2) * 64, (lane >> 2) * 36864 + (lane & 3) * 16);
    });
    sfor<9>([&](auto I) { rda(IC<9 + I>{}); });
    waitv<3>();
    barl();
    if (w >= 1 && w < 4) pfA();
    ld4<0>(dd, (const char*)ssd, ((lane == 0 ? chA : lane == 1 ? chA + 4 : ch3) >> 1) * 4);
    TS(6);
    pin(xm); pin(xf);
    *(LDS3 u32x4*)(L + w * 2048 + vz) = xm; *(LDS3 u32x2*)(L + w * 2048 + 1024 + lane * 8) = xf;
    const u32x2 x0 = *(LDS3 u32x2*)(L + w * 2048 + c * 64 + q * 8), x1 = *(LDS3 u32x2*)(L + w * 2048 + c * 64 + q * 8 + 32), xe = *(LDS3 u32x2*)(L + w * 2048 + 1024 + c * 32 + q * 8);
    f32x4 a0 = {}, a1 = {}, ae = {}, am = {};
    const unsigned bb = L + L_HS + tf * 18432, tm = L + L_RM + q * 16;
    auto gm = [&](auto X) {
      bf16x8 B[18], M[18], E[18];
      auto rd = [&](auto I) { B[I] = (I == 4 || I == 13) ? splitfrag(bb + I * 1024 + lane * 8) : ldsfrag(bb + I * 1024 + vz); M[I] = ldsfrag(tm + I * 64); if constexpr (X) E[I] = trfrag(te + (L_EX2 - L_EXT) + I * 1024, 512); };
      sfor<3>(rd);
      sfor<18>([&](auto I) {
        if constexpr (I + 3 < 18) rd(IC<I + 3>{});
        a0 = mfma(A0[I], B[I], a0); a1 = mfma(A1[I], B[I], a1); am = mfma(M[I], B[I], am);
        if constexpr (X) ae = mfma(E[I], B[I], ae);
        SB();
      });
    };
    if (ext) gm(IC<1>{}); else gm(IC<0>{});
    TS(7);
    pin(st);
    const f32x2 sf = __builtin_bit_cast(f32x2, st); const float m = sf[0], rs = sf[1], mu = am[0];
    float ss = 0;
    auto E = [&](u32x2 xv, f32x4 a) {
      const float v0 = (blo(xv[0]) - m) * rs + a[0] - mu, v1 = (bhi(xv[0]) - m) * rs + a[1] - mu, v2 = (blo(xv[1]) - m) * rs + a[2] - mu, v3 = (bhi(xv[1]) - m) * rs + a[3] - mu;
      ss += (v0 * v0 + v1 * v1) + (v2 * v2 + v3 * v3);
      return u32x2{pk2(v0, v1), pk2(v2, v3)};
    };
    const char* xc = ws + O_XC + (size_t)(2 * T32 + tf) * 18 * 1024;
    const u32x2 o0 = E(x0, a0), o1 = E(x1, a1);
    st16c(xc + (4 * g2 + p) * 1024, vz, u32x4{o0[0], o0[1], o1[0], o1[1]});
    if (ext) st8c(xc + (32 + g2) * 512, lane * 8, E(xe, ae));
    ss = sumq(ss);
    if (lane < 16) { st4c(ws + O_SS + (2 * T32 + tf) * 1024 + (4 * g2 + p) * 64, lane * 4, __builtin_bit_cast(unsigned, ss)); if (g2 == 0 && p == 0) st4c(ws + O_MU, t * 4, __builtin_bit_cast(unsigned, mu)); }
    waitv<0>();
    pin(dd);
    if (w == 4 && lane < 3) {
      LDS3 unsigned short* hp = (LDS3 unsigned short*)(L + L_CV + lane * CV_SZ + CV_H);
      *hp = (unsigned short)(pk2(blo(*hp) + (((lane < 2 ? chA : ch3) & 1) ? bhi(dd) : blo(dd)), 0.f) & 0xffff);
    }
    barl();
    if (tid == 0) st4c(fl + F2, fid, tag);
    TS(8);
  }
  f32x4 ys[2];
  {
    const int st = 9 + c - 8 * q;
    const unsigned sh = (st & 1) * 2, ho = L + L_CV + CV_H + (st >> 1) * 4, uo = L + L_CV + CV_U + (c - 1) * 32 + q * 16;
    f32x4 acc[8] = {}, c0 = {}, c1 = {};
    unsigned f2;
    sfor<2>([&](auto S) {
      const unsigned ha = ho + tf * CV_SZ + (2 * p + S) * 64, ua = uo + tf * CV_SZ - (2 * p + S) * 64;
      bf16x8 A[8], B[8];
      A[0] = toep(ha, sh);
      sfor<8>([&](auto M) { B[M] = ldsfrag(ua + M * 512); });
      sfor<8>([&](auto M) {
        if constexpr (M < 4) A[M + 1] = toep(ha + (M + 1) * 512, sh); else if constexpr (M == 4) sfor<3>([&](auto I) { A[5 + I] = toep(ha + (5 + I) * 512, sh); });
        sfor<8 - M>([&](auto I) { acc[M + I] = mfma(A[M], B[I], acc[M + I]); });
        if constexpr (M < 6 - 3 * S) dma(L + (6 * S + M) * 8192 + w * 1024, wa + (size_t)(6 * S + M) * 32 * 9216, va);
        if constexpr (S * 8 + M == EF) ld4c<0>(f2, fl + F2 + T * 128, c * 4);
        if constexpr (S * 8 + M == PB3) { if (w >= 5) pfB(); }
        if constexpr (S * 8 + M == P4C) { if (w >= 2) pf(L, wk + xi * 41472, min(tid - 128, 323) * 128); }
      });
    });
    auto zis = [&]() {
    if (EF == P4C && w >= 2) waitv<1>(); else waitv<0>();
    pin(f2);
    if (!allq(f2, tag)) spin<unsigned>(fl + F2 + T * 128, c * 4, tag);
    sfor<3>([&](auto I) { dmac(L + (9 + I) * 8192 + w * 1024, zs + I * 1024, vz); });
    };
    if constexpr (EF < 0) ld4c<0>(f2, fl + F2 + T * 128, c * 4); else if constexpr (ZE) zis();
    { const unsigned ha = ho + 2 * CV_SZ + w * 64, ua = uo + 2 * CV_SZ - w * 64, za = L + L_CV + (lane & 31) * 16;
      sfor<8>([&](auto M) {
        const bf16x8 A = toep(ha + M * 512, sh);
        c1 = mfma(A, ldsfrag(M < 5 || 7 - g >= M ? ua + (7 - g - M) * 512 : za), c1);
        if constexpr (M < 4) c0 = mfma(A, ldsfrag(g >= M ? ua + (g - M) * 512 : za), c0);
      }); }
    TS(9);
    if constexpr (EF < 0 || !ZE) zis();
    barl();
    TS(10);
    const unsigned ra = L + L_RB + tf * 24576 + vz;
    sfor<4>([&](auto P) { if (p == P) {
      sfor<6>([&](auto I) { constexpr int r = 1 + (I >> 1), d = P ^ r; *(LDS3 f32x4*)(ra + (d * 3 + r - 1) * 2048 + (I & 1) * 1024) = acc[2 * d + (I & 1)]; });
      ys[0] = acc[2 * P]; ys[1] = acc[2 * P + 1];
    } });
    const unsigned rc = L + L_RB + 49152 + vz;
    if (w < 7) { *(LDS3 f32x4*)(rc + w * 2048) = c0; *(LDS3 f32x4*)(rc + w * 2048 + 1024) = c1; }
    barl();
    sfor<6>([&](auto I) { ys[I & 1] += *(LDS3 f32x4*)(ra + p * 6144 + I * 1024); });
    if (w == 7) sfor<7>([&](auto I) { c0 += *(LDS3 f32x4*)(rc + I * 2048); c1 += *(LDS3 f32x4*)(rc + I * 2048 + 1024); });
    pin(ys[0]); pin(ys[1]); pin(c0); pin(c1);
    waitv<1>();
    barl();
    if (w == 7) {
      st8c(ws + O_YT + (size_t)ch3 * 4096 + g * 512, c * 32 + q * 8, u32x2{pk2(c0[0], c0[1]), pk2(c0[2], c0[3])});
      st8c(ws + O_YT + (size_t)ch3 * 4096 + (7 - g) * 512, c * 32 + q * 8, u32x2{pk2(c1[0], c1[1]), pk2(c1[2], c1[3])});
    }
  }
  unsigned sq[8];
  sfor<6>([&](auto I) { dmac(L + (12 + I) * 8192 + w * 1024, zs + (3 + I) * 1024, vz); });
  TS(14);
  sfor<8>([&](auto I) { ld4c<(I >> 2) * 1024 + (I & 3) * 64>(sq[I], ws + O_SS + (8 * T + 2 * p) * 1024, q * 256 + c * 4); });
  st16c(ws + O_YT + (size_t)(chA + 4 * tf) * 4096 + p * 1024, c * 64 + q * 16, u32x4{pk2(ys[0][0], ys[0][1]), pk2(ys[0][2], ys[0][3]), pk2(ys[1][0], ys[1][1]), pk2(ys[1][2], ys[1][3])});
  const char* wd = w3 + 2304 * g + 2048 + 64 * s;
  const int rb = 8 * w + (lane >> 3), rd = 16 * w + (lane >> 2);
  const unsigned vb = rb * 9216 + (((lane & 7) ^ (((rb >> 1) & 3) << 1)) * 16), vd = rd * 9216 + (((lane & 3) ^ (((rd >> 2) & 1) << 1)) * 16);
  auto issue = [&](auto N) {
    constexpr int n = N; const unsigned dst = L + phys(n) * 8192 + w * 1024;
    if constexpr (n < 36) { if constexpr (n & 1) dma(dst, wa + (size_t)(n >> 1) * 32 * 9216, va); else dmac(dst, zs + (n >> 1) * 1024, vz); }
    else if constexpr (n < 45) dma(dst, wa + 256 + (size_t)(n - 36) * 64 * 9216, vb);
    else if constexpr (n < 54) dma(dst, wa + 384 + (size_t)(n - 45) * 64 * 9216, vb);
    else if constexpr (n < 58) dma(dst, wd + (size_t)(n - 54) * 128 * 9216, vd);
    else dma(dst, wd + (size_t)(tf ? 448 : 512) * 9216, vd);
  };
  const char* hb = ws + O_H + ((size_t)(8 * T + 2 * p) * 144 + 36 * g) * 1024;
  auto ks4 = [&](int j) { return j < 8 ? 8 * ((s + (j >> 1)) & 3) + 2 * (j & 1) : j < 16 ? 8 * ((s + j) & 3) + (j >> 2) * 2 : 2 * j; };
  unsigned ta[8], tz = L + 73728 + p * 2048 + vz, t4e, fv[4];
  u32x4 fc;
  ta[0] = L + row * 256 + (((4 * tf) ^ (row & 7)) * 32) + (c & 3) * 8;
  sfor<4>([&](auto I) { ta[I] = ta[0] ^ (I * 32); pin(ta[I]); }); pin(tz);
  bf16x8 Z[18][2], A[4], B[16][2];
  u32x2 Bs[2][2][2];
  f32x4 acc[9][2], ac4[9][2];
  auto rd_ = [&](auto I) {
    constexpr int i = I, wi = i >> 2, ph = phys(endidx(wi) - 1), hi = ph >= 9, so = (ph % 9) * 8192;
    if constexpr (i == 72) sfor<4>([&](auto K) { ta[K] = ((L + row * 128 + (((2 * tf) ^ ((row >> 1) & 3)) * 32) + (c & 3) * 8) ^ ((K & 1) * 32)) + (K >> 1) * 73728; pin(ta[K]); });
    if constexpr (i == 144) sfor<2>([&](auto K) { ta[K] = L + row * 64 + ((tf ^ ((row >> 2) & 1)) * 32) + (c & 3) * 8 + K * 73728; pin(ta[K]); });
    if constexpr (i == 162) { sfor<8>([&](auto K) { ta[K] = (L + tf * 73728 + row * 256 + (((row & 7) ^ (2 * g)) * 32) + (c & 3) * 8) ^ (K * 32); pin(ta[K]); }); t4e = L + L_SP + tf * 1024 + row * 32 + (c & 3) * 8; pin(t4e); }
    if constexpr (i < 72) {
      if constexpr ((i & 3) == 0) sfor<2>([&](auto K) { Z[wi][K] = wi < 16 ? ldsfrag(tz + so + K * 1024) : splitfrag(tz - lane * 8 + so + K * 1024); });
      A[i & 3] = trfrag(ta[i & 3] + so, 4096);
    } else if constexpr (i < 144) A[i & 3] = trfrag(ta[2 * hi + (i & 1)] + so + ((i >> 1) & 1) * 4096, 2048);
    else if constexpr (i < 162) A[i & 3] = trfrag(ta[hi] + so + (i & 3) * 2048, 1024);
    else { constexpr int j = (i - 162) / 9, u = (i - 162) % 9; if constexpr (u < 8) A[i & 3] = trfrag(ta[u] + ((3 + j) & 7) * 8192, 4096); else A[i & 3] = trfrag(t4e + (j & 7) * 2048, 512); }
  };
  TS(11);
  sfor<4>(rd_);
  float ca[2], cb[2], sg[2], gp;
  unsigned pk[4];
  auto gl = [&](auto V) {
    constexpr int v = V, pr = v >> 4, t2 = v < 64 ? (v >> 3) & 1 : (v >> 2) & 1, tl = v < 64 ? 2 * pr + ((v >> 2) & 1) : 8;
    const float o = gelu(acc[tl][t2][v & 3], ca[t2], cb[t2], sg[t2]);
    if constexpr (v & 1) pk[(v >> 1) & 3] = pk2(gp, o); else gp = o;
    if constexpr (v < 64 && (v & 7) == 7) st16c(hb + (t2 * 144 + 8 * s + (pr < 2 ? 2 * tf + pr : 2 * pr + tf)) * 1024, vz, u32x4{pk[0], pk[1], pk[2], pk[3]});
    if constexpr (v >= 64 && (v & 3) == 3) st8c(hb + (t2 * 144 + 32 + s) * 1024 + tf * 512, lane * 8, u32x2{pk[2 * t2], pk[2 * t2 + 1]});
  };
  const int fr = 8 * T + 2 * p + tf, t = 16 * fr + c, n0 = 128 * s + 32 * g + 4 * q, ne = 512 + 16 * s + 4 * q;
  unsigned yv[12], mu; u32x2 xf, st, ce, pe[3]; u32x4 xm, cm, pm[3];
  unsigned xo, xo2;
  auto finld = [&](auto I) {
    if constexpr (I < 12) ld4c<0>(yv[I], ws + O_YT + ((I & 3) + (I >> 2 == 1 ? 16 : 0)) * 4096, I < 8 ? n0 * 4096 + (fr >> 5) * 1024 + (fr & 15) * 64 + (fr & 16) / 2 + (c >> 2) * 16 + (c & 2) * 2 : ne * 4096 + (t >> 1) * 4);
    else if constexpr (I == 12) { unsigned l = vz; pin(l); xo = (16 * fr + (l >> 6)) * 1152; xo2 = xo + (l & 48) / 2; xo += l & 48; ld16<0>(xm, x + 256 * s + 64 * g, xo); }
    else if constexpr (I == 13) ld8<1024>(xf, x + 32 * s, xo2);
    else if constexpr (I == 15) ld8c<0>(st, ws + O_XST, t * 8);
    else if constexpr (I == 16) ld4c<0>(mu, ws + O_MU, t * 4);
    else if constexpr (I == 14) ld16c(cm, ws + O_XC + (size_t)(fr * 18 + 4 * s + g) * 1024, vz);
    else ld8c<0>(ce, ws + O_XC + (size_t)(fr * 36 + 32 + s) * 512, lane * 8);
  };
  auto step = [&](auto I) {
    constexpr int i = I, j = (i - 162) / 9, u = (i - 162) % 9;
    if constexpr (i < 162 ? (i + 4) % 8 == 0 : (u == 5 && j < 17)) {
      waitv<vcnt(i)>(); bar();
      if constexpr (i >= 156) { constexpr int jn = i < 162 ? 0 : j + 1; if constexpr (jn < 16) { pin(B[jn][0]); pin(B[jn][1]); } else sfor<4>([&](auto K) { pin(Bs[jn - 16][K >> 1][K & 1]); }); }
      if constexpr (i == PUB[0]) { if (w == 1 && vz < NR * 16) st4c(fl + FC + b * 4, vz * (RS / 16), tag); }
      sfor<4>([&](auto E) {
        if constexpr (PUB[E] == i) { if (tid == 0) st4c(fl + F3 + E * 2048, fid, tag); }
        if constexpr (EXM[E] == i) { pin(fv[E]); if (!allq(fv[E], tag)) spin<unsigned>(fl + F3 + E * 2048, f3o, tag); }
      });
      if constexpr (EXM[4] == i) { pin(fc); if (!allq(fc, tag)) spin<u32x4>(fl + FC + (b >> 3 & (NR - 1)) * RS, vz, tag); }
    }
    const f32x4 z4 = {};
    if constexpr (i < 162) {
      constexpr int ks = i < 72 ? i >> 2 : i < 108 ? (i - 72) >> 1 : i < 144 ? (i - 108) >> 1 : i - 144;
      constexpr int tl = i < 72 ? i & 3 : i < 108 ? 4 + (i & 1) : i < 144 ? 6 + (i & 1) : 8;
      sfor<2>([&](auto K) { acc[tl][K] = mfma(A[i & 3], Z[ks][K], ks == 0 ? z4 : acc[tl][K]); });
    } else sfor<2>([&](auto K) {
      bf16x8 bf;
      if constexpr (j < 16) bf = B[j][K]; else bf = __builtin_bit_cast(bf16x8, __builtin_shufflevector(Bs[j - 16][K][0], Bs[j - 16][K][1], 0, 1, 2, 3));
      ac4[u][K] = mfma(A[i & 3], bf, j == 0 ? z4 : ac4[u][K]);
    });
    if constexpr (i + 4 < 324) rd_(IC<i + 4>{});
    if constexpr (j == 17 && u >= 2) *(LDS3 f32x4*)(L + (3 + u) * 8192 + w * 1024 + vz) = ac4[u - 2][1];
    if constexpr (i >= 4 && i < 162) { constexpr int G = (i + 4) / 8, k = (i + 4) % 8; if constexpr ((k & 1) && lim3(G - 1) + (k >> 1) < lim3(G)) issue(IC<lim3(G - 1) + (k >> 1)>{}); }
    sfor<36>([&](auto N) {
      constexpr int jj = N >> 1, k = N & 1;
      if constexpr (wIt(jj, k) == i) dma(L + (((3 + jj) & 7) + 9 * k) * 8192 + w * 1024, wk + 256 * s + (ks4(jj) + k) * 36864, vo);
      if constexpr (bIt(jj, k) == i) {
        const char* hp = hb + ((k ^ tf) * 144 + ks4(jj) + tf) * 1024;
        if constexpr (jj < 16) ld16c(B[jj][k], hp, vz); else { ld8c<0>(Bs[jj - 16][k][0], hp, lane * 8); ld8c<512>(Bs[jj - 16][k][1], hp, lane * 8); }
      }
    });
    sfor<5>([&](auto E) {
      if constexpr (eIt(E) == i) dma(L + L_SP + (E & 1) * 8192 + w * 1024, wk + 1024 + 32 * s + (ks4(E < 4 ? 4 * E + (w >> 1) : 16 + (p >> 1)) + (w & 1)) * 36864, ve);
      if constexpr (LDF[E] == i) { if constexpr (E < 4) ld4c<0>(fv[E], fl + F3 + E * 2048, f3o); else ld16c(fc, fl + FC + (b >> 3 & (NR - 1)) * RS, vz); }
    });
    if constexpr (i >= FIN && i < FIN + 18) finld(IC<i - FIN>{});
    if constexpr (i == PF4) { unsigned l = vz; pin(l); pf(L, wk + xi * 41472, min((int)l * 8 + w * 8192, 323 * 128)); }
    if constexpr (i >= CLR && i <= CLR + 12 && (i - CLR) % 4 == 0) {
      const u32x4 n4 = {~0u, ~0u, ~0u, ~0u};
      if constexpr (i < CLR + 12) st16c(ws + O_P + (size_t)((b * 3 + (i - CLR) / 4) * 8 + w) * 1024, vz, n4);
      else if (w < 3) st16c(ws + O_PE + (size_t)(b * 3 + w) * 1024, vz, n4);
    }
    if constexpr (i == 36) TX(12);
    if constexpr (i == 72) TX(13);
    if constexpr (i == 108) TX(14);
    if constexpr (i == 144) TX(15);
    if constexpr (i == 162) TS(16);
    if constexpr (i == 216) TX(17);
    if constexpr (i == 270) TX(18);
    if constexpr (i == 60) sfor<2>([&](auto J) {
      float y = 0;
      sfor<4>([&](auto K) { pin(sq[4 * J + K]); y += __builtin_bit_cast(float, sq[4 * J + K]); });
      const float vr = sumq(y) * (1.f / D_) + EPS, r = __builtin_amdgcn_rsqf(vr); sg[J] = vr * r; ca[J] = -GK * r; cb[J] = -GK * GC * r * r * r;
    });
    if constexpr (gV(i) >= 0) gl(IC<gV(i)>{});
    SB();
  };
  if (PRI && tf) __builtin_amdgcn_s_setprio(1);
  sfor<162>(step);
  sfor<162>([&](auto I) { step(IC<162 + I>{}); });
  TS(19);
  sfor<2>([&](auto U) { *(LDS3 f32x4*)(L + (14 + U) * 8192 + w * 1024 + vz) = ac4[7 + U][1]; });
  barl();
  f32x4 z[9];
  sfor<9>([&](auto V) { constexpr int U = (V + 2) % 9; z[U] = ac4[U][0] + *(LDS3 f32x4*)(L + (U < 7 ? 5 + U : 7 + U) * 8192 + (w ^ 4) * 1024 + vz); });
  auto pz = [&](float a, float d) { const unsigned u = pk2(a, d); return u < 0xfffffffeu ? u : 0xfffffffeu; };
  sfor<3>([&](auto R) { constexpr int r = R + 1; st16c(ws + O_P + (size_t)(((b ^ r) * 3 + R) * 8 + w) * 1024, vz, u32x4{pz(z[2 * r][0], z[2 * r][1]), pz(z[2 * r][2], z[2 * r][3]), pz(z[2 * r + 1][0], z[2 * r + 1][1]), pz(z[2 * r + 1][2], z[2 * r + 1][3])}); });
  const bool own = p == g;
  if (!own) st8c(ws + O_PE + (size_t)(((b ^ p ^ g) * 3 + (p ^ g) - 1) * 2 + tf) * 512, lane * 8, u32x2{pz(z[8][0], z[8][1]), pz(z[8][2], z[8][3])});
  TS(20);
  waitv<3>(); pin(xm); pin(xf);
  const unsigned lt = L + w * 2048, lq = lt + c * 64 + q * 8;
  *(LDS3 u32x4*)(lt + vz) = xm; *(LDS3 u32x2*)(lt + 1024 + lane * 8) = xf;
  u32x2 xa[3] = {*(LDS3 u32x2*)lq, *(LDS3 u32x2*)(lq + 32), *(LDS3 u32x2*)(lt + 1024 + c * 32 + q * 8)};
  sfor<3>([&](auto I) { pin(xa[I]); });
  sfor<3>([&](auto I) { pm[I] = u32x4{0, 0, 0, 0}; pe[I] = u32x2{0, 0}; });
  unsigned mk = __builtin_amdgcn_readfirstlane(own ? 15 : 7);
  if constexpr (DLY) waitv<0>();
#pragma unroll 1
  for (int it = 0; it < (1 << 17) && mk; ++it) {
    asm volatile("s_bitcmp1_b32 %12, 0\n\ts_cbranch_scc0 1f\n\tglobal_load_dwordx4 %0, %6, %8 sc1\n1:\n\ts_bitcmp1_b32 %12, 1\n\ts_cbranch_scc0 2f\n\tglobal_load_dwordx4 %1, %6, %9 sc1\n2:\n\t"
                 "s_bitcmp1_b32 %12, 2\n\ts_cbranch_scc0 3f\n\tglobal_load_dwordx4 %2, %6, %10 sc1\n3:\n\ts_bitcmp1_b32 %12, 3\n\ts_cbranch_scc0 4f\n\t"
                 "global_load_dwordx2 %3, %7, %11 sc1\n\tglobal_load_dwordx2 %4, %7, %11 offset:1024 sc1\n\tglobal_load_dwordx2 %5, %7, %11 offset:2048 sc1\n4:\n\ts_waitcnt vmcnt(0)"
                 : "+v"(pm[0]), "+v"(pm[1]), "+v"(pm[2]), "+v"(pe[0]), "+v"(pe[1]), "+v"(pe[2])
                 : "v"(vz), "v"(lane * 8), "s"(ws + O_P + (size_t)(b * 24 + w) * 1024), "s"(ws + O_P + (size_t)(b * 24 + 8 + w) * 1024), "s"(ws + O_P + (size_t)(b * 24 + 16 + w) * 1024), "s"(ws + O_PE + (size_t)(b * 6 + tf) * 512), "s"(mk) : "scc", "memory");
    unsigned mx[4] = {};
    sfor<12>([&](auto I) { const unsigned u = pm[I >> 2][I & 3]; mx[I >> 2] = u > mx[I >> 2] ? u : mx[I >> 2]; });
    sfor<6>([&](auto I) { const unsigned u = pe[I >> 1][I & 1]; mx[3] = u > mx[3] ? u : mx[3]; });
    mk = 0;
    sfor<4>([&](auto I) { mk |= __builtin_amdgcn_ballot_w64(mx[I] == ~0u) ? 1u << I : 0u; });
    mk = __builtin_amdgcn_readfirstlane(mk);
  }
  TS(22);
  sfor<12>([&](auto I) { pin(yv[I]); }); pin(st); pin(mu); pin(cm); pin(ce);
  const f32x2 sf = __builtin_bit_cast(f32x2, st);
  auto fin = [&](auto H) {
    float r[4];
    sfor<4>([&](auto E) {
      constexpr int e = E, k = 2 * H + (e >> 1);
      auto X = [&](unsigned u) { return (e & 1) ? bhi(u) : blo(u); };
      float ob = __builtin_bit_cast(float, mu) + z[H < 2 ? H : 8][e];
      if constexpr (H < 2) ob += (X(cm[k]) + X(pm[0][k])) + (X(pm[1][k]) + X(pm[2][k])); else ob += (X(ce[k - 4]) + X(pe[0][k - 4])) + (X(pe[1][k - 4]) + X(pe[2][k - 4]));
      const float xf = X(xa[H][e >> 1]), y = (c & 1) ? bhi(yv[4 * H + e]) : blo(yv[4 * H + e]);
      r[e] = xf + (xf - sf[0]) * sf[1] * y * (ob * __builtin_amdgcn_rcpf(1.f + __builtin_amdgcn_exp2f(-1.44269504f * ob)));
    });
    *(LDS3 u32x2*)(H < 2 ? lq + 32 * H : lt + 1024 + c * 32 + q * 8) = u32x2{pk2(r[0], r[1]), pk2(r[2], r[3])};
  };
  fin(IC<0>{}); fin(IC<1>{});
  *(u32x4*)(out + 256 * s + 64 * g + xo) = *(LDS3 u32x4*)(lt + vz);
  if (own) { fin(IC<2>{}); *(u32x2*)(out + 1024 + 32 * s + xo2) = *(LDS3 u32x2*)(lt + 1024 + lane * 8); }
  TS(23);
  TSEND;
}

extern "C" void kernel_launch(void* const* d_in, const int* in_sizes, int n_in, void* d_out, int out_size, void* d_ws, size_t ws_size, hipStream_t stream) {
  k_all<<<256, 512, 0, stream>>>((const char*)d_in[0], (const char*)d_in[1], (const char*)d_in[2], (const char*)d_in[3], (const char*)d_in[4], (const unsigned*)d_in[5],
                                 (const char*)d_in[7], (const char*)d_in[8], (const char*)d_in[9], (const char*)d_in[10], (char*)d_ws, (char*)d_out);
}
```
